```python
import torch

def _softmax(x, axis=-1):
    return torch.softmax(x, dim=axis)
def _gelu(x):
    return torch.nn.functional.gelu(x, approximate="tanh")
def _silu(x):
    return torch.nn.functional.silu(x)
def _sigmoid(x):
    return torch.sigmoid(x)
def _layernorm(x, axis=-1, eps=1e-5):
    m = x.mean(dim=axis, keepdim=True)
    v = x.var(dim=axis, keepdim=True, unbiased=False)
    return (x - m) / torch.sqrt(v + eps)
def _scatter_add(tgt, idx, src, axis=0):
    return tgt.clone().index_add_(axis, idx, src)
def _segment_sum(src, seg, n):
    out = torch.zeros((n,) + src.shape[1:], dtype=src.dtype)
    return out.index_add_(0, seg, src)
def _pad(x, pw):
    flat = tuple(v for pair in reversed(pw) for v in pair)
    return torch.nn.functional.pad(x, flat)

kernel_name = 'parallel_sum__linear_attn_block__mixer_token_mlp__prompt'

def setup_inputs():

    _w = (lambda a: (lambda t: t.to(torch.bfloat16) if t.is_floating_point() else t)(torch.as_tensor(a)))
    def _A_setup():
        B, S, D = 1, 2048, 576
        return {
            "x": _w((lambda *s: torch.randn(*s, dtype=torch.bfloat16) * 0.05)(B, S, D)),
            "wqkv": _w((lambda *s: torch.randn(*s, dtype=torch.bfloat16) * 0.05)(D, 3 * D)),
            "wo": _w((lambda *s: torch.randn(*s, dtype=torch.bfloat16) * 0.05)(D, D)),
        }
    _A_in = _A_setup()

    def _B_setup():
        B, S, D, F = 1, 2048, 576, 4608
        return {
            "x": _w((lambda *s: torch.randn(*s, dtype=torch.bfloat16) * 0.05)(B, S, D)),
            "w1": _w((lambda *s: torch.randn(*s, dtype=torch.bfloat16) * 0.4)(S, F) * (S ** -0.5)),
            "b1": _w((lambda *s: torch.randn(*s, dtype=torch.bfloat16) * 3.2)(F) * 0.02),
            "w2": _w((lambda *s: torch.randn(*s, dtype=torch.bfloat16) * 0.2)(F, S) * (F ** -0.5)),
            "b2": _w((lambda *s: torch.randn(*s, dtype=torch.bfloat16) * 0.4)(S) * 0.02),
        }
    _B_in = _B_setup()

    return {
        "A_x": _A_in["x"],
        "A_wqkv": _A_in["wqkv"],
        "A_wo": _A_in["wo"],
        "B_x": _B_in["x"],
        "B_w1": _B_in["w1"],
        "B_b1": _B_in["b1"],
        "B_w2": _B_in["w2"],
        "B_b2": _B_in["b2"],
    }

def reference(A_x, A_wqkv, A_wo, B_x, B_w1, B_b1, B_w2, B_b2):
    def _A_ref(x, wqkv, wo):
        B, S, D = x.shape
        H = 32; Dh = D // H
        qkv = (lambda x, *d: x.permute(*d))((x @ wqkv).reshape(B, S, 3, H, Dh), 2, 0, 3, 1, 4)
        q, k, v = qkv[0], qkv[1], qkv[2]
        phi = lambda t: torch.where(t > 0, t + 1, torch.exp(t)) + 1e-6
        pq, pk = phi(q), phi(k)
        kv = torch.einsum("bhsd,bhse->bhde", pk, v)
        z = torch.einsum("bhsd->bhd", pk)
        num = torch.einsum("bhqd,bhde->bhqe", pq, kv)
        den = torch.einsum("bhqd,bhd->bhq", pq, z)[..., None] + 1e-6
        o = num / den
        return (lambda x, *d: x.permute(*d))(o, 0, 2, 1, 3).reshape(B, S, D) @ wo

    def _B_ref(x, w1, b1, w2, b2):
        B, S, D = x.shape
        h = _layernorm(x, axis=-1)
        u = torch.einsum("bsd,sf->bdf", h, w1) + b1
        u = _gelu(u)
        y = torch.einsum("bdf,fs->bds", u, w2) + b2
        return (lambda x, *d: x.permute(*d))(y, 0, 2, 1)

    x = A_x
    xn = _layernorm(x, axis=-1)
    return x + _A_ref(xn, A_wqkv, A_wo) + _B_ref(xn, B_w1, B_b1, B_w2, B_b2)
```

```cpp
#include <hip/hip_runtime.h>
#include <hip/hip_bf16.h>
typedef unsigned short u16;
__device__ inline float bf2f(u16 b){ return __uint_as_float(((unsigned)b)<<16); }
__device__ inline u16 f2bf(float f){ unsigned u=__float_as_uint(f); u += 0x7FFFu + ((u>>16)&1u); return (u16)(u>>16); }
__device__ inline float rbf(float f){ return bf2f(f2bf(f)); }

constexpr int S=2048, D=576, F=4608, H=32, DH=18;

__device__ inline float wsum(float v){ for(int o=32;o>0;o>>=1) v+=__shfl_xor(v,o); return v; }

__global__ void ln_k(const u16* x, u16* xn, u16* h){
  int row = blockIdx.x*4 + (threadIdx.x>>6); int lane=threadIdx.x&63;
  float v[9]; float s=0;
  for(int i=0;i<9;i++){ v[i]=bf2f(x[row*D+lane+64*i]); s+=v[i]; }
  s=wsum(s); float m=s/D; float q=0;
  for(int i=0;i<9;i++){ float d=v[i]-m; q+=d*d; }
  q=wsum(q)/D; float mb=rbf(m), vb=rbf(q); float den=rbf(sqrtf(rbf(vb+1e-5f)));
  s=0;
  for(int i=0;i<9;i++){ v[i]=rbf(rbf(v[i]-mb)/den); xn[row*D+lane+64*i]=f2bf(v[i]); s+=v[i]; }
  s=wsum(s); m=s/D; q=0;
  for(int i=0;i<9;i++){ float d=v[i]-m; q+=d*d; }
  q=wsum(q)/D; mb=rbf(m); vb=rbf(q); den=rbf(sqrtf(rbf(vb+1e-5f)));
  for(int i=0;i<9;i++){ h[row*D+lane+64*i]=f2bf(rbf(v[i]-mb)/den); }
}
template<int MODE>
__global__ void gemm_k(const u16* A,int sam,int sak,const u16* B,int sbk,int sbn,int M,int N,int K,
                       u16* C, const u16* e0, const u16* e1, const u16* e2){
  __shared__ float As[16][65], Bs[16][65];
  int tx=threadIdx.x&15, ty=threadIdx.x>>4; int m0=blockIdx.y*64, n0=blockIdx.x*64;
  float acc[4][4]={};
  for(int k0=0;k0<K;k0+=16){
    for(int i=threadIdx.x;i<1024;i+=256){ int kk,mm; if(sak==1){kk=i&15;mm=i>>4;} else {mm=i&63;kk=i>>6;}
      As[kk][mm]=bf2f(A[(long)(m0+mm)*sam+(long)(k0+kk)*sak]); }
    for(int i=threadIdx.x;i<1024;i+=256){ int kk,nn; if(sbk==1){kk=i&15;nn=i>>4;} else {nn=i&63;kk=i>>6;}
      Bs[kk][nn]=bf2f(B[(long)(k0+kk)*sbk+(long)(n0+nn)*sbn]); }
    __syncthreads();
    #pragma unroll
    for(int kk=0;kk<16;kk++){ float a[4],b[4];
      for(int i=0;i<4;i++){a[i]=As[kk][ty*4+i]; b[i]=Bs[kk][tx*4+i];}
      for(int i=0;i<4;i++)for(int j=0;j<4;j++)acc[i][j]+=a[i]*b[j]; }
    __syncthreads();
  }
  for(int i=0;i<4;i++)for(int j=0;j<4;j++){ int m=m0+ty*4+i,n=n0+tx*4+j; float v=acc[i][j];
    if(MODE==0) C[(long)m*N+n]=f2bf(v);
    if(MODE==1){ float u=rbf(rbf(v)+bf2f(e0[n])); float t=0.7978845608f*(u+0.044715f*u*u*u); float g=0.5f*u*(1.f+tanhf(t)); C[(long)m*N+n]=f2bf(g);}
    if(MODE==2){
      float y=rbf(rbf(v)+bf2f(e0[n])); long o=(long)n*D+m; C[o]=f2bf(rbf(bf2f(e1[o])+bf2f(e2[o]))+y); }
  }
}
__global__ void phi_k(u16* qkv){ long i=(long)blockIdx.x*256+threadIdx.x; int n=i%(2*D); long s=i/(2*D);
  float t=bf2f(qkv[s*3*D+n]); float p= t>0? rbf(t+1.f): rbf(expf(t)); qkv[s*3*D+n]=f2bf(p+1e-6f); }
__global__ void kv_k(const u16* qkv, float* kv, float* z){
  int h=blockIdx.x; int t=threadIdx.x; if(t<DH*DH){ int d=t/DH,e=t%DH; float a=0; for(int s=0;s<S;s++) a+=bf2f(qkv[s*3*D+D+h*DH+d])*bf2f(qkv[s*3*D+2*D+h*DH+e]); kv[h*DH*DH+t]=rbf(a);}
  else if(t<DH*DH+DH){ int d=t-DH*DH; float a=0; for(int s=0;s<S;s++) a+=bf2f(qkv[s*3*D+D+h*DH+d]); z[h*DH+d]=rbf(a);} }
__global__ void o_k(const u16* qkv,const float* kv,const float* z,u16* o){ long i=(long)blockIdx.x*256+threadIdx.x; int c=i%D; long s=i/D; int h=c/DH,e=c%DH;
  float num=0,den=0; for(int d=0;d<DH;d++){ float p=bf2f(qkv[s*3*D+h*DH+d]); num+=p*kv[h*DH*DH+d*DH+e]; den+=p*z[h*DH+d]; }
  num=rbf(num); den=rbf(rbf(den)+1e-6f); o[i]=f2bf(num/den); }

extern "C" void kernel_launch(void* const* d_in, const int* in_sizes, int n_in, void* d_out, int out_size, void* d_ws, size_t ws_size, hipStream_t stream){
  const u16* x=(const u16*)d_in[0]; const u16* wqkv=(const u16*)d_in[1]; const u16* wo=(const u16*)d_in[2];
  const u16* w1=(const u16*)d_in[4]; const u16* b1=(const u16*)d_in[5]; const u16* w2=(const u16*)d_in[6]; const u16* b2=(const u16*)d_in[7];
  char* ws=(char*)d_ws; u16* xn=(u16*)ws; u16* h=xn+S*D; u16* qkv=h+S*D; u16* G=qkv+S*3*D; u16* o=G+D*F; u16* at=o+S*D; float* kv=(float*)(at+S*D); float* z=kv+H*DH*DH;
  ln_k<<<S/4,256,0,stream>>>(x,xn,h);
  gemm_k<0><<<dim3(3*D/64,S/64),256,0,stream>>>(xn,D,1,wqkv,3*D,1,S,3*D,D,qkv,nullptr,nullptr,nullptr);
  phi_k<<<S*2*D/256,256,0,stream>>>(qkv);
  kv_k<<<H,384,0,stream>>>(qkv,kv,z);
  o_k<<<S*D/256,256,0,stream>>>(qkv,kv,z,o);
  gemm_k<0><<<dim3(D/64,S/64),256,0,stream>>>(o,D,1,wo,D,1,S,D,D,at,nullptr,nullptr,nullptr);
  gemm_k<1><<<dim3(F/64,D/64),256,0,stream>>>(h,1,D,w1,F,1,D,F,S,G,b1,nullptr,nullptr);
  gemm_k<2><<<dim3(S/64,D/64),256,0,stream>>>(G,F,1,w2,S,1,D,S,F,(u16*)d_out,b2,x,at);
}
```

```cpp
#include <hip/hip_runtime.h>
#include <stdint.h>

namespace {
typedef unsigned short u16;
typedef short s16x4 __attribute__((ext_vector_type(4)));
typedef short s16x8 __attribute__((ext_vector_type(8)));
typedef __bf16 bf16x8 __attribute__((ext_vector_type(8)));
typedef float f32x4 __attribute__((ext_vector_type(4)));
typedef unsigned u32x2 __attribute__((ext_vector_type(2)));
typedef unsigned u32x4 __attribute__((ext_vector_type(4)));
#define LAS __attribute__((address_space(3)))
#define GAS __attribute__((address_space(1)))
#define DI __device__ __forceinline__
typedef __amdgpu_buffer_rsrc_t rsrc_t;

constexpr int S = 2048, D = 576, F = 4608;
constexpr unsigned OFF_FLAGS = 0;
constexpr unsigned OFF_HT   = 65536;
constexpr unsigned OFF_XN   = OFF_HT + 2359296;
constexpr unsigned OFF_GX   = OFF_XN + 2359296;
constexpr unsigned GX_BYTES = 648u * 576u * 16u;
constexpr unsigned OFF_PQ   = OFF_GX + GX_BYTES;
constexpr unsigned OFF_PQS  = OFF_PQ + 2359296;
constexpr unsigned OFF_KVS  = OFF_PQS + 2359296;
constexpr unsigned KVS_SLAB = 2736;
constexpr unsigned OFF_PART = OFF_KVS + 64u * KVS_SLAB * 4u;
constexpr unsigned PART_BYTES = 64u * 16u * 4u * 5u * 1024u;
constexpr unsigned OFF_STAMP = OFF_PART + PART_BYTES;
constexpr unsigned FL_PREP = 0;
constexpr unsigned FL_XF = 256;
constexpr unsigned FL_TMO = 4400;
constexpr int SPIN_CAP = 1 << 20;

#define PF_ROWS 1024

DI float bf2f(unsigned b) { return __uint_as_float(b << 16); }
typedef __bf16 bf16x2 __attribute__((ext_vector_type(2)));
DI unsigned cvt_pk(float lo, float hi) { const bf16x2 v = {(__bf16)lo, (__bf16)hi}; return __builtin_bit_cast(unsigned, v); }
DI float lo_f(unsigned p) { return __uint_as_float(p << 16); }
DI float hi_f(unsigned p) { return __uint_as_float(p & 0xffff0000u); }
DI float rbf(float x) { return lo_f(cvt_pk(x, x)); }
DI rsrc_t mk_rsrc(const void* p, unsigned bytes) { return __builtin_amdgcn_make_buffer_rsrc((void*)p, (short)0, (int)bytes, 0x00020000); }
template <int AUX>
DI void dma16(rsrc_t r, unsigned voff, unsigned soff, unsigned ldsaddr) {
  if (AUX == 0) asm volatile("s_mov_b32 m0, %0\n\ts_nop 0\n\tbuffer_load_dwordx4 %1, %2, %3 offen lds" ::"s"(ldsaddr), "v"(voff), "s"(r), "s"(soff) : "memory");
  else          asm volatile("s_mov_b32 m0, %0\n\ts_nop 0\n\tbuffer_load_dwordx4 %1, %2, %3 offen sc1 lds" ::"s"(ldsaddr), "v"(voff), "s"(r), "s"(soff) : "memory");
}
template <int N> DI void wait_vm() { asm volatile("s_waitcnt vmcnt(%0)" ::"i"(N) : "memory"); }
DI void wait_lgkm0() { asm volatile("s_waitcnt lgkmcnt(0)" ::: "memory"); }
DI void bar() { asm volatile("" ::: "memory"); __builtin_amdgcn_s_barrier(); asm volatile("" ::: "memory"); }
DI void barl() { wait_lgkm0(); bar(); }
DI unsigned rfl(unsigned x) { return __builtin_amdgcn_readfirstlane(x); }
DI s16x8 lds_rd128(unsigned a) { return *(const LAS s16x8*)(size_t)a; }
DI s16x8 lds_rdtr(unsigned a0, unsigned a1) {
  s16x4 lo = __builtin_amdgcn_ds_read_tr16_b64_v4i16((LAS s16x4*)(size_t)a0);
  s16x4 hi = __builtin_amdgcn_ds_read_tr16_b64_v4i16((LAS s16x4*)(size_t)a1);
  return __builtin_shufflevector(lo, hi, 0, 1, 2, 3, 4, 5, 6, 7);
}
DI f32x4 mfma16(s16x8 a, s16x8 b, f32x4 c) { return __builtin_amdgcn_mfma_f32_16x16x32_bf16(__builtin_bit_cast(bf16x8, a), __builtin_bit_cast(bf16x8, b), c, 0, 0, 0); }
template <class T> DI T lds_ld(unsigned a) { return *(const LAS T*)(size_t)a; }
template <class T> DI void lds_st(unsigned a, T v) { *(LAS T*)(size_t)a = v; }

#define STAMP(ws, slot, k)
#define STAMPW(ws, slot, k)

template <int CTRL> DI float dpp_add(float v) { return v + __int_as_float(__builtin_amdgcn_update_dpp(0, __float_as_int(v), CTRL, 0xf, 0xf, true)); }
DI float half_sum(float v) {
  v = dpp_add<0xB1>(v); v = dpp_add<0x4E>(v); v = dpp_add<0x141>(v); v = dpp_add<0x140>(v);
  auto r = __builtin_amdgcn_permlane16_swap(__float_as_uint(v), __float_as_uint(v), false, false);
  return __uint_as_float(r[0]) + __uint_as_float(r[1]);
}
DI void ln_row(float (&v)[18]) {
  float s = 0.f;
#pragma unroll
  for (int i = 0; i < 18; i++) s += v[i];
  const float m = half_sum(s) * (1.0f / 576.0f);
  float q = 0.f;
#pragma unroll
  for (int i = 0; i < 18; i++) { float d = v[i] - m; q += d * d; }
  const float var = half_sum(q) * (1.0f / 576.0f);
  const float mb = rbf(m), den = rbf(sqrtf(rbf(rbf(var) + 1e-5f)));
  const float inv = 1.0f / den;
#pragma unroll
  for (int i = 0; i < 18; i += 2) {
    unsigned p = cvt_pk(v[i] - mb, v[i + 1] - mb);
    p = cvt_pk(lo_f(p) * inv, hi_f(p) * inv);
    v[i] = lo_f(p); v[i + 1] = hi_f(p);
  }
}

__global__ __launch_bounds__(256) void k1_ln(const u16* __restrict__ x, const char* __restrict__ wqkv, const char* __restrict__ w1, char* __restrict__ ws) {
  __shared__ __attribute__((aligned(16))) char sm[2 * 8 * 1168];
  const int tid = threadIdx.x, lane = tid & 63, v = tid >> 6, hh = lane >> 5, l = lane & 31;
  const int b = blockIdx.x, xcd = b & 7, r = b >> 3, w = xcd * 32 + r, kb = w >> 2, g = w & 3;
  STAMP(ws, b, 0);
  const int j = 2 * v + hh, row = 32 * kb + 4 * g + (j & 3) + 16 * (j >> 2);
  unsigned xr[9];
  const unsigned* xp = (const unsigned*)(x + (size_t)row * D) + l;
#pragma unroll
  for (int i = 0; i < 9; i++) xr[i] = xp[32 * i];
  u32x4 pf[2 + PF_ROWS / 32 * 72 / 256 + 1];
  {
#pragma unroll
    for (int k = 0; k < 2; k++) { unsigned o = (unsigned)((w * 2 + k) * 256 + tid) * 16u; pf[k] = *(const u32x4*)(wqkv + (o < 576u * 3456u ? o : 0u)); }
#pragma unroll
    for (int k = 0; k < PF_ROWS / 32 * 72 / 256 + (PF_ROWS ? 1 : 0); k++) {
      unsigned idx = k * 256 + tid; unsigned rr = idx / 72, c = idx - rr * 72; rr = rr * 32 + r;
      pf[2 + k] = *(const u32x4*)(w1 + (rr < PF_ROWS ? (size_t)rr * 9216 + xcd * 1152 + c * 16 : 0));
    }
  }
  if (tid < 32) ((unsigned*)(ws + OFF_FLAGS))[w * 32 + tid] = 0u;
  float f[18];
#pragma unroll
  for (int i = 0; i < 9; i++) { f[2 * i] = lo_f(xr[i]); f[2 * i + 1] = hi_f(xr[i]); }
  const unsigned xs = (unsigned)(size_t)(LAS char*)sm, hs = xs + 8 * 1168;
  ln_row(f);
#pragma unroll
  for (int i = 0; i < 9; i++) lds_st<unsigned>(xs + j * 1168 + (32 * i + l) * 4, (__float_as_uint(f[2 * i]) >> 16) | (__float_as_uint(f[2 * i + 1]) & 0xffff0000u));
  ln_row(f);
#pragma unroll
  for (int i = 0; i < 9; i++) lds_st<unsigned>(hs + j * 1168 + (32 * i + l) * 4, (__float_as_uint(f[2 * i]) >> 16) | (__float_as_uint(f[2 * i + 1]) & 0xffff0000u));
  __syncthreads();
#pragma unroll
  for (int m = 0; m < 2; m++) {
    const int dp = tid + 256 * m;
    if (dp < 288) {
      unsigned a[8];
#pragma unroll
      for (int jj = 0; jj < 8; jj++) a[jj] = lds_ld<unsigned>(hs + jj * 1168 + dp * 4);
      u32x4 c0, c1;
#pragma unroll
      for (int k = 0; k < 4; k++) { c0[k] = __builtin_amdgcn_perm(a[2 * k + 1], a[2 * k], 0x05040100u); c1[k] = __builtin_amdgcn_perm(a[2 * k + 1], a[2 * k], 0x07060302u); }
      u32x4* o = (u32x4*)(ws + OFF_HT + ((size_t)(kb * 4 + g) * D + 2 * dp) * 16);
      o[0] = c0; o[1] = c1;
    }
  }
#pragma unroll
  for (int m = 0; m < 3; m++) {
    const int idx = tid + 256 * m;
    if (idx < 576) {
      const int qc = idx >> 3, jj = idx & 7, d0 = 32 * (qc >> 2) + 4 * (qc & 3);
      u32x2 a = lds_ld<u32x2>(xs + jj * 1168 + d0 * 2), c = lds_ld<u32x2>(xs + jj * 1168 + d0 * 2 + 32);
      const int rw = 32 * kb + 4 * g + (jj & 3) + 16 * (jj >> 2);
      *(u32x4*)(ws + OFF_XN + ((size_t)qc * S + rw) * 16) = (u32x4){a.x, a.y, c.x, c.y};
    }
  }
#pragma unroll
  for (int k = 0; k < 2 + PF_ROWS / 32 * 72 / 256 + (PF_ROWS ? 1 : 0); k++) asm volatile("" ::"v"(pf[k]));
  STAMP(ws, b, 1);
}

template <int XR_, int WRB_, int NS_> struct Ring {
  static constexpr int XR = XR_, WRB = WRB_, NS = NS_;
  static constexpr int XBYTES = 8 * XR * 16, WBYTES = 64 * WRB, STG = XBYTES + WBYTES;
  static constexpr int XP = XBYTES / 1024, WP = WBYTES / 1024;
  static constexpr int XQ = (XP + 3) / 4, WQ = WP / 4;
  static constexpr int BYTES = NS * STG;
};
typedef Ring<144, 256, 4> RingA;
typedef Ring<128, 384, 3> RingB;

template <class R> struct LoadOff { unsigned x[R::XQ]; unsigned w[R::WQ]; };
template <class R> DI void load_off(LoadOff<R>& o, int lw, int lane, int xld  , int x0, int wld  , int w0  ) {
#pragma unroll
  for (int q = 0; q < R::XQ; q++) { int idx = 64 * (lw + 4 * q) + lane; int cr = idx / R::XR, m = idx - cr * R::XR; o.x[q] = (unsigned)((cr * xld + x0 + m) * 16); }
#pragma unroll
  for (int q = 0; q < R::WQ; q++) {
    int idx = 64 * (lw + 4 * q) + lane;
    if (R::WRB == 256) { int row = idx >> 4, c = (idx & 15) ^ ((row & 7) << 1); o.w[q] = (unsigned)(row * wld + w0 + c * 16); }
    else { int row = idx / 24, cp = idx - row * 24, c = cp ^ (((row >> 1) & 3) << 1); o.w[q] = (unsigned)(row * wld + w0 + c * 16); }
  }
}
template <class R, int NX, bool MAYSC1, class Src>
DI void loader_loop(const LoadOff<R>& o, const Src& src, int lw, int n, int nextra) {
  constexpr int P = NX + R::WQ, NS = R::NS;
  auto issue = [&](int s, int slot) {
    rsrc_t rx, rw; unsigned sx, sw; bool sc1;
    src.get(s, rx, sx, rw, sw, sc1);
    const unsigned lb = (unsigned)slot * R::STG + lw * 1024u;
    if (MAYSC1 && sc1) {
#pragma unroll
      for (int q = 0; q < NX; q++) dma16<1>(rx, o.x[q], sx, lb + q * 4096u);
#pragma unroll
      for (int q = 0; q < R::WQ; q++) dma16<1>(rw, o.w[q], sw, lb + R::XBYTES + q * 4096u);
    } else {
#pragma unroll
      for (int q = 0; q < NX; q++) dma16<0>(rx, o.x[q], sx, lb + q * 4096u);
#pragma unroll
      for (int q = 0; q < R::WQ; q++) dma16<0>(rw, o.w[q], sw, lb + R::XBYTES + q * 4096u);
    }
  };
#pragma unroll
  for (int s = 0; s < NS - 1; s++) issue(s, s);
  for (int e = 0; e < nextra; e++) bar();
  int slot = NS - 1;
  for (int i = 0; i < n - (NS - 1); i++) {
    wait_vm<P*(NS - 2)>();
    bar();
    issue(i + NS - 1, slot); slot = (slot + 1 == NS) ? 0 : slot + 1;
  }
  if (NS >= 4) { wait_vm<P * ((NS >= 4) ? 2 : 0)>(); bar(); }
  wait_vm<P>(); bar();
  wait_vm<0>(); bar();
}
template <class R, bool MAYSC1, class Src>
DI void loader(const LoadOff<R>& o, const Src& src, int lw, int n, int nextra) {
  if (R::XP % 4 == 0) loader_loop<R, R::XQ, MAYSC1>(o, src, lw, n, nextra);
  else if (lw < R::XP % 4) loader_loop<R, R::XQ, MAYSC1>(o, src, lw, n, nextra);
  else loader_loop<R, R::XQ - 1, MAYSC1>(o, src, lw, n, nextra);
}

template <class R, int XB, int WB, bool XA> struct Mma {
  f32x4 acc[XB][WB];
  unsigned xa;
  static constexpr int NWA = (R::WRB == 256) ? WB : (WB < 4 ? WB : 4);
  unsigned wa[NWA];
  int wrot;
  DI void init(int lane, int xb0, int wb0) {
    const int g = lane >> 4, l15 = lane & 15, q = l15 >> 2, p = lane & 3;
    xa = (unsigned)((g * R::XR + xb0 * 16 + l15) * 16);
    const unsigned wrow = (unsigned)(R::XBYTES + (4 * g + q) * R::WRB + p * 8);
    if (R::WRB == 256) {
      const int r7 = (4 * g + q) & 7;
#pragma unroll
      for (int u = 0; u < NWA; u++) wa[u] = wrow + (((wb0 + u) ^ r7) * 32);
    } else {
      const int f = ((4 * g + q) >> 1) & 3;
#pragma unroll
      for (int u = 0; u < NWA; u++) { const int cb = wb0 + u; wa[u] = wrow + (cb >> 2) * 128 + (((cb & 3) ^ f) * 32); }
    }
    zero();
  }
  DI void zero() {
#pragma unroll
    for (int x = 0; x < XB; x++)
#pragma unroll
      for (int u = 0; u < WB; u++) acc[x][u] = (f32x4){0.f, 0.f, 0.f, 0.f};
  }
  struct Frag { s16x8 x[XB]; s16x8 w[WB]; };
  DI void rd(Frag& fr, unsigned so, int kk) const {
#pragma unroll
    for (int u = 0; u < WB; u++) {
      const unsigned a = (R::WRB == 256 ? wa[u] : wa[u & 3] + (u >> 2) * 128) + so + kk * 32 * R::WRB;
      fr.w[u] = lds_rdtr(a, a + 16 * R::WRB);
    }
#pragma unroll
    for (int x = 0; x < XB; x++) fr.x[x] = lds_rd128(xa + so + kk * (4 * R::XR * 16) + x * 256);
  }
  DI void mm(const Frag& fr) {
#pragma unroll
    for (int x = 0; x < XB; x++)
#pragma unroll
      for (int u = 0; u < WB; u++) acc[x][u] = XA ? mfma16(fr.x[x], fr.w[u], acc[x][u]) : mfma16(fr.w[u], fr.x[x], acc[x][u]);
  }
  static DI void ilv() {
    constexpr int NR = XB + 2 * WB, NM = XB * WB, NI = NR < NM ? NR : NM;
#pragma unroll
    for (int z = 0; z < NI; z++) {
      __builtin_amdgcn_sched_group_barrier(0x8, 1, 0); __builtin_amdgcn_sched_group_barrier(0x100, 1, 0);
      if (NR > NM && z < NR - NM) __builtin_amdgcn_sched_group_barrier(0x100, 1, 0);
    }
    if constexpr (NM > NI) __builtin_amdgcn_sched_group_barrier(0x8, NM - NI, 0);
  }
};
template <class R, bool LAST, class M, class Hook>
DI void mma_pass(M& m, typename M::Frag& f0, unsigned& so, int n, const Hook& hook) {
  typename M::Frag f1;
  const int nfull = LAST ? n - 1 : n;
  for (int i = 0; i < nfull; i++) {
    m.rd(f1, so, 1);
    m.mm(f0);
    M::ilv();
    __builtin_amdgcn_sched_barrier(0);
    hook(i);
    wait_lgkm0();
    bar();
    __builtin_amdgcn_sched_barrier(0);
    so += R::STG; if (so == R::NS * R::STG) so = 0;
    m.rd(f0, so, 0);
    m.mm(f1);
    M::ilv();
    __builtin_amdgcn_sched_barrier(0);
  }
  if (LAST) { m.rd(f1, so, 1); m.mm(f0); M::ilv(); __builtin_amdgcn_sched_barrier(0); m.mm(f1); }
}
struct NoHook { DI void operator()(int) const {} };

struct SrcLin { rsrc_t rx, rw; unsigned xstep, wstep;
  DI void get(int s, rsrc_t& a, unsigned& sx, rsrc_t& b, unsigned& sw, bool& sc1) const { a = rx; b = rw; sx = s * xstep; sw = s * wstep; sc1 = false; } };
struct SrcKV { rsrc_t rx, rw;
  DI void get(int s, rsrc_t& a, unsigned& sx, rsrc_t& b, unsigned& sw, bool& sc1) const {
    const int p = s >= 9, k = s - 9 * p; a = rx; b = rw; sx = k * (8 * S * 16); sw = k * (64 * 3456) + p * 1152; sc1 = false; } };

DI float phi(float t) {
  const float e = __builtin_amdgcn_exp2f(t * 1.4426950408889634f);
  return (t > 0.f ? t + 1.0f : e) + 1e-6f;
}
DI unsigned phi_pk(float a, float b) { unsigned p = cvt_pk(a, b); return cvt_pk(phi(lo_f(p)), phi(hi_f(p))); }

DI void k2_gemm1(int wid, int lane, int ft, int dt, const u16* w1, const u16* b1, char* ws) {
  typedef RingA R;
  if (wid >= 4) {
    LoadOff<R> o; load_off<R>(o, wid - 4, lane, D, dt * 144, F * 2, ft * 256);
    SrcLin src{mk_rsrc(ws + OFF_HT, S * D * 2), mk_rsrc(w1, (unsigned)S * F * 2), 8 * D * 16, 64 * F * 2};
    loader<R, false>(o, src, wid - 4, S / 64, 0);
  } else {
    typedef Mma<R, 9, 2, false> M;
    M m; m.init(lane, 0, 2 * wid);
    typename M::Frag f0; unsigned so = 0;
    bar();
    m.rd(f0, so, 0);
    mma_pass<R, true>(m, f0, so, S / 64, NoHook());
    const int g = lane >> 4, l15 = lane & 15;
    float bb[2][4];
#pragma unroll
    for (int u = 0; u < 2; u++) { u32x2 v = *(const u32x2*)(b1 + ft * 128 + 32 * wid + 16 * u + 4 * g); bb[u][0] = lo_f(v.x); bb[u][1] = hi_f(v.x); bb[u][2] = lo_f(v.y); bb[u][3] = hi_f(v.y); }
    const float c0 = -2.0f * 0.7978845608028654f * 1.4426950408889634f, c1 = c0 * 0.044715f;
    char* gp = ws + OFF_GX + ((size_t)((ft * 4 + wid) * 4 + g) * D + dt * 144 + l15) * 16;
#pragma unroll
    for (int x = 0; x < 9; x++) {
      float gv[8];
#pragma unroll
      for (int u = 0; u < 2; u++)
#pragma unroll
        for (int rr = 0; rr < 4; rr++) {
          const float v = m.acc[x][u][rr] + bb[u][rr];
          gv[u * 4 + rr] = v * __builtin_amdgcn_rcpf(1.0f + __builtin_amdgcn_exp2f(v * (c0 + c1 * v * v)));
        }
      *(u32x4*)(gp + x * 256) = (u32x4){cvt_pk(gv[0], gv[1]), cvt_pk(gv[2], gv[3]), cvt_pk(gv[4], gv[5]), cvt_pk(gv[6], gv[7])};
    }
  }
}

DI void k2_q(int wid, int lane, int i, int u3, const u16* wqkv, char* ws) {
  typedef RingB R;
  if (wid >= 4) {
    LoadOff<R> o; load_off<R>(o, wid - 4, lane, S, i * 128, 3456, u3 * 384);
    SrcLin src{mk_rsrc(ws + OFF_XN, S * D * 2), mk_rsrc(wqkv, 576u * 3456u), 8 * S * 16, 64 * 3456};
    loader<R, false>(o, src, wid - 4, 9, 0);
  } else {
    typedef Mma<R, 4, 6, true> M;
    const int xh = wid & 1, wh = wid >> 1;
    M m; m.init(lane, 4 * xh, 0);
    {
      const int g = lane >> 4, l15 = lane & 15, q = l15 >> 2, p = lane & 3, f = ((4 * g + q) >> 1) & 3;
      const unsigned wrow = (unsigned)(R::XBYTES + (4 * g + q) * 384 + p * 8);
#pragma unroll
      for (int u = 0; u < 4; u++) { const int cb = 6 * wh + u; m.wa[u] = wrow + (cb >> 2) * 128 + (((cb & 3) ^ f) * 32); }
    }
    typename M::Frag f0; unsigned so = 0;
    bar();
    m.rd(f0, so, 0);
    mma_pass<R, true>(m, f0, so, 9, NoHook());
    const int g = lane >> 4, l15 = lane & 15;
#pragma unroll
    for (int x = 0; x < 4; x++)
#pragma unroll
      for (int u = 0; u < 6; u++) {
        const f32x4 a = m.acc[x][u];
        const int sb = i * 8 + 4 * xh + x, c = u3 * 192 + (6 * wh + u) * 16 + l15;
        *(u32x2*)(ws + OFF_PQ + ((size_t)sb * D + c) * 32 + g * 8) = (u32x2){phi_pk(a[0], a[1]), phi_pk(a[2], a[3])};
      }
  }
}

DI void k2_kv(int wid, int lane, int i, int j, const u16* wqkv, char* ws) {
  typedef RingB R;
  if (wid >= 4) {
    LoadOff<R> o; load_off<R>(o, wid - 4, lane, S, i * 128, 3456, 1152 + j * 256);
    SrcKV src{mk_rsrc(ws + OFF_XN, S * D * 2), mk_rsrc(wqkv, 576u * 3456u)};
    loader<R, false>(o, src, wid - 4, 18, 0);
  } else {
    typedef Mma<R, 2, 9, true> M;
    M m; m.init(lane, 2 * wid, j);
    typename M::Frag f0; unsigned so = 0;
    bar();
    m.rd(f0, so, 0);
    mma_pass<R, false>(m, f0, so, 9, NoHook());
    constexpr unsigned KOFF = R::BYTES;
#pragma unroll
    for (int u = 0; u < 9; u++) {
      const f32x4 a = m.acc[0][u], c = m.acc[1][u];
      lds_st<u32x4>(KOFF + u * 4096 + wid * 1024 + lane * 16, (u32x4){phi_pk(a[0], a[1]), phi_pk(a[2], a[3]), phi_pk(c[0], c[1]), phi_pk(c[2], c[3])});
    }
    m.zero();
    mma_pass<R, true>(m, f0, so, 9, NoHook());
    barl();
    constexpr unsigned VOFF = 0;
#pragma unroll
    for (int u = 0; u < 9; u++) {
      const f32x4 a = m.acc[0][u], c = m.acc[1][u];
      lds_st<u32x4>(VOFF + u * 4096 + wid * 1024 + lane * 16, (u32x4){cvt_pk(a[0], a[1]), cvt_pk(a[2], a[3]), cvt_pk(c[0], c[1]), cvt_pk(c[2], c[3])});
    }
    barl();
    const int g = lane >> 4, l15 = lane & 15;
    float* slab = (float*)(ws + OFF_KVS) + (size_t)(i * 4 + j) * KVS_SLAB;
    const s16x8 ones = {0x3f80, 0x3f80, 0x3f80, 0x3f80, 0x3f80, 0x3f80, 0x3f80, 0x3f80};
    for (int bi = wid; bi < 9; bi += 4) {
      f32x4 dz = {0.f, 0.f, 0.f, 0.f}, d0 = dz, d1 = dz, d2 = dz;
      const int bl = bi > 0 ? bi - 1 : 0, bh = bi < 8 ? bi + 1 : 8;
#pragma unroll
      for (int sw = 0; sw < 4; sw++) {
        const s16x8 a = lds_rd128(KOFF + bi * 4096 + sw * 1024 + lane * 16);
        dz = mfma16(a, ones, dz);
        d0 = mfma16(a, lds_rd128(VOFF + bl * 4096 + sw * 1024 + lane * 16), d0);
        d1 = mfma16(a, lds_rd128(VOFF + bi * 4096 + sw * 1024 + lane * 16), d1);
        d2 = mfma16(a, lds_rd128(VOFF + bh * 4096 + sw * 1024 + lane * 16), d2);
      }
#pragma unroll
      for (int rr = 0; rr < 4; rr++) {
        const int dd = 16 * bi + 4 * g + rr, h = dd / 18, dl = dd - 18 * h;
        if (l15 == 0) slab[2592 + dd] = dz[rr];
        int e = 16 * (bi - 1) + l15 - 18 * h; if (bi > 0 && e >= 0 && e < 18) slab[h * 324 + dl * 18 + e] = d0[rr];
        e = 16 * bi + l15 - 18 * h;           if (e >= 0 && e < 18) slab[h * 324 + dl * 18 + e] = d1[rr];
        e = 16 * (bi + 1) + l15 - 18 * h;     if (bi < 8 && e >= 0 && e < 18) slab[h * 324 + dl * 18 + e] = d2[rr];
      }
    }
  }
}

constexpr int LDS2 = RingB::BYTES + 9 * 4096;
static_assert(LDS2 >= RingA::BYTES && LDS2 <= 163840, "lds");
__global__ __launch_bounds__(512, 2) void k2_mid(const u16* __restrict__ wqkv, const u16* __restrict__ w1, const u16* __restrict__ b1, char* __restrict__ ws) {
  extern __shared__ __attribute__((aligned(16))) char smem[];
  const int tid = threadIdx.x, wid = rfl(tid >> 6), lane = tid & 63;
  const int b = blockIdx.x, xcd = b & 7, r = b >> 3;
  STAMP(ws, 256 + b, 0);
  if (r < 18) { const int gi = xcd * 18 + r; k2_gemm1(wid, lane, gi >> 2, gi & 3, w1, b1, ws); }
  else if (r < 26) k2_kv(wid, lane, 8 * (xcd >> 2) + (r - 18), xcd & 3, wqkv, ws);
  else k2_q(wid, lane, 2 * xcd + ((r - 26) & 1), (r - 26) >> 1, wqkv, ws);
  STAMP(ws, 256 + b, 1);
}

struct SrcG2 { rsrc_t rx, rw, rp; int j;
  DI void get(int s, rsrc_t& a, unsigned& sx, rsrc_t& b, unsigned& sw, bool& sc1) const {
    const int st = j == 0 ? (s < 12 ? s : s + 60) : 20 * j - 8 + s;
    a = rx; sx = st * (8 * D * 16); sc1 = st >= 72;
    if (st >= 72) { b = rp; sw = (st - 72) * (64 * S * 2); } else { b = rw; sw = st * (64 * S * 2); } } };

DI unsigned ld_relaxed(const unsigned* p) { return __hip_atomic_load((const GAS unsigned*)p, __ATOMIC_RELAXED, __HIP_MEMORY_SCOPE_AGENT); }
DI void st_relaxed(unsigned* p, unsigned v) { __hip_atomic_store((GAS unsigned*)p, v, __ATOMIC_RELAXED, __HIP_MEMORY_SCOPE_AGENT); }

constexpr unsigned PREP_LDS = RingA::BYTES;
constexpr unsigned PL_PQ = PREP_LDS, PL_Z = PL_PQ + 9216, PL_RD = PL_Z + 576, PL_KV = PL_RD + 1024, LDS3 = PL_KV + 1440 + 32;

template <int J> DI constexpr int own_lo() { return J == 0 ? 0 : J == 1 ? 4 : J == 2 ? 8 : 13; }
template <int J> DI constexpr int own_n() { return J < 2 ? 4 : 5; }

template <int J, class M>
DI void k3_finish(M& m, int wid, int lane, int i, int t, const u16* x, const u16* b2, u16* out, char* ws) {
  const int T = i * 4 + t, g = lane >> 4, l15 = lane & 15;
  unsigned* fl = (unsigned*)(ws + OFF_FLAGS);
  const rsrc_t rp = mk_rsrc(ws + OFF_PART, PART_BYTES);
  const unsigned vo = (unsigned)(wid * 5 * 1024 + lane * 16);
#pragma unroll
  for (int b = 0; b < 18; b++) {
    const int dst = b < 4 ? 0 : b < 8 ? 1 : b < 13 ? 2 : 3;
    if (dst != J) {
      const int idx = b - (dst == 0 ? 0 : dst == 1 ? 4 : dst == 2 ? 8 : 13);
      __builtin_amdgcn_raw_buffer_store_b128(__builtin_bit_cast(u32x4, m.acc[b >> 1][b & 1]), rp, vo + idx * 1024, (unsigned)(((T * 4 + dst) * 4 + J) * 20480), 16);
    }
  }
  constexpr int LO = own_lo<J>(), N = own_n<J>();
  u32x2 xv[N]; float bv[2];
#pragma unroll
  for (int u = 0; u < 2; u++) bv[u] = bf2f(b2[i * 128 + 32 * wid + 16 * u + l15]);
#pragma unroll
  for (int k = 0; k < N; k++) { const int b = LO + k; xv[k] = *(const u32x2*)(x + (size_t)(i * 128 + 32 * wid + 16 * (b & 1) + l15) * D + t * 144 + 16 * (b >> 1) + 4 * g); }
  wait_vm<0>();
  bar();
  if (wid == 0) {
    if (lane == 0) st_relaxed(fl + FL_XF + (T * 4 + J) * 16, 1u);
    for (int spin = 0;; spin++) {
      const unsigned v = lane < 4 && lane != J ? ld_relaxed(fl + FL_XF + (T * 4 + lane) * 16) : 1u;
      if (__all(v == 1u)) break;
      if (spin > SPIN_CAP) { if (lane == 0) st_relaxed(fl + FL_TMO, 1u); break; }
      __builtin_amdgcn_s_sleep(1);
    }
  }
  bar();
  u32x4 p[3][N];
#pragma unroll
  for (int sr = 0, z = 0; sr < 4; sr++) if (sr != J) {
#pragma unroll
    for (int k = 0; k < N; k++) p[z][k] = __builtin_amdgcn_raw_buffer_load_b128(rp, vo + k * 1024, (unsigned)(((T * 4 + J) * 4 + sr) * 20480), 16);
    z++;
  }
#pragma unroll
  for (int k = 0; k < N; k++) {
    const int b = LO + k;
    f32x4 s = {0.f, 0.f, 0.f, 0.f};
#pragma unroll
    for (int sr = 0, z = 0; sr < 4; sr++) { if (sr == J) s += m.acc[b >> 1][b & 1]; else { s += __builtin_bit_cast(f32x4, p[z][k]); z++; } }
    const float bb = bv[b & 1];
    *(u32x2*)(out + (size_t)(i * 128 + 32 * wid + 16 * (b & 1) + l15) * D + t * 144 + 16 * (b >> 1) + 4 * g) =
        (u32x2){cvt_pk(lo_f(xv[k].x) + s[0] + bb, hi_f(xv[k].x) + s[1] + bb), cvt_pk(lo_f(xv[k].y) + s[2] + bb, hi_f(xv[k].y) + s[3] + bb)};
  }
}

__global__ __launch_bounds__(512, 2) void k3_out(const u16* __restrict__ x, const u16* __restrict__ wo, const u16* __restrict__ w2, const u16* __restrict__ b2, u16* __restrict__ out, char* __restrict__ ws) {
  extern __shared__ __attribute__((aligned(16))) char smem[];
  typedef RingA R;
  const int tid = threadIdx.x, wid = rfl(tid >> 6), lane = tid & 63;
  const int b = blockIdx.x, xcd = b & 7, r = b >> 3, j = xcd >> 1, i = 8 * (xcd & 1) + (r >> 2), t = r & 3;
  const int n = j == 0 ? 21 : 20;
  STAMP(ws, 512 + b, 0);
  if (wid >= 4) {
    LoadOff<R> o; load_off<R>(o, wid - 4, lane, D, t * 144, S * 2, i * 256);
    SrcG2 src{mk_rsrc(ws + OFF_GX, GX_BYTES), mk_rsrc(w2, (unsigned)F * S * 2), mk_rsrc(ws + OFF_PQS, S * D * 2), j};
    loader<R, true>(o, src, wid - 4, n, 2);
  } else {
    unsigned* fl = (unsigned*)(ws + OFF_FLAGS);
    const float* kvs = (const float*)(ws + OFF_KVS);
    const int w = xcd * 32 + r, jp = w & 3, sb2 = w >> 2;
    const int ua = (j - 1) * 64 + i * 4 + t, h = ua / 6, dr = ua - 6 * h;
    u32x4 pq[3];
#pragma unroll
    for (int m3 = 0; m3 < 3; m3++) { const int idx = tid + 256 * m3; const int mm = idx / 288, rem = idx - mm * 288;
      pq[m3] = *(const u32x4*)(ws + OFF_PQ + ((size_t)(2 * sb2 + (idx < 576 ? mm : 0)) * D + 144 * jp) * 32 + (idx < 576 ? rem : 0) * 16); }
    float zs = 0.f, kv0 = 0.f, kv1 = 0.f; u16 wov[18];
    if (tid < 144) {
#pragma unroll
      for (int s = 0; s < 16; s++) zs += kvs[(size_t)(s * 4 + jp) * KVS_SLAB + 2592 + tid];
    }
    if (j > 0) {
      const float* kp = kvs + (size_t)(h >> 3) * KVS_SLAB + (h & 7) * 324;
#pragma unroll
      for (int s = 0; s < 16; s++) { kv0 += kp[(size_t)s * 4 * KVS_SLAB + tid]; }
      if (tid < 68) {
#pragma unroll
        for (int s = 0; s < 16; s++) kv1 += kp[(size_t)s * 4 * KVS_SLAB + 256 + tid];
      }
      if (tid < 192) {
#pragma unroll
        for (int e = 0; e < 18; e++) wov[e] = wo[(size_t)(18 * h + e) * D + 96 * dr + (tid >= 96 ? tid - 96 : tid)];
      }
    }
#pragma unroll
    for (int m3 = 0; m3 < 3; m3++) { const int idx = tid + 256 * m3; if (idx < 576) { const int mm = idx / 288, rem = idx - mm * 288; lds_st<u32x4>(PL_PQ + (rem >> 1) * 64 + mm * 32 + (rem & 1) * 16, pq[m3]); } }
    if (tid < 144) lds_st<float>(PL_Z + tid * 4, rbf(zs));
    if (j > 0) { { const int c = tid / 18; lds_st<float>(PL_KV + (c * 20 + tid - 18 * c) * 4, rbf(kv0)); }
      if (tid < 68) { const int tt = tid + 256, c = tt / 18; lds_st<float>(PL_KV + (c * 20 + tt - 18 * c) * 4, rbf(kv1)); } }
    barl();
    { const int hh = tid >> 5, s = tid & 31; float den = 0.f;
#pragma unroll
      for (int dd = 0; dd < 18; dd++) den += bf2f(lds_ld<u16>(PL_PQ + (18 * hh + dd) * 64 + s * 2)) * lds_ld<float>(PL_Z + (18 * hh + dd) * 4);
      lds_st<float>(PL_RD + (hh * 32 + s) * 4, 1.0f / rbf(rbf(den) + 1e-6f)); }
    if (j > 0 && tid < 192) {
      const int half = tid >= 96, d = 96 * dr + tid - 96 * half;
      float wf[18];
#pragma unroll
      for (int e = 0; e < 18; e++) wf[e] = bf2f(wov[e]);
      for (int cc = 0; cc < 9; cc++) {
        const int c = 9 * half + cc; float a = 0.f;
#pragma unroll
        for (int e4 = 0; e4 < 16; e4 += 4) { const f32x4 kq = lds_ld<f32x4>(PL_KV + (c * 20 + e4) * 4); a += kq[0] * wf[e4] + kq[1] * wf[e4 + 1] + kq[2] * wf[e4 + 2] + kq[3] * wf[e4 + 3]; }
        a += lds_ld<float>(PL_KV + (c * 20 + 16) * 4) * wf[16] + lds_ld<float>(PL_KV + (c * 20 + 17) * 4) * wf[17];
        const int k = F + 18 * h + c;
        u16* dst = (u16*)(ws + OFF_GX + ((size_t)((k >> 5) * 4 + ((k & 15) >> 2)) * D + d) * 16 + (((k >> 4) & 1) * 4 + (k & 3)) * 2);
        __hip_atomic_store((GAS u16*)dst, (u16)(cvt_pk(a, a) & 0xffffu), __ATOMIC_RELAXED, __HIP_MEMORY_SCOPE_AGENT);
      }
    }
    barl();
    { const rsrc_t rq = mk_rsrc(ws + OFF_PQS, S * D * 2);
#pragma unroll
      for (int m3 = 0; m3 < 3; m3++) { const int idx = tid + 256 * m3;
        if (idx < 576) { const int c = idx >> 2, qd = idx & 3, hh = c / 18;
          const u32x4 v = lds_ld<u32x4>(PL_PQ + c * 64 + qd * 16);
          const f32x4 r0 = lds_ld<f32x4>(PL_RD + (hh * 32 + qd * 8) * 4), r1 = lds_ld<f32x4>(PL_RD + (hh * 32 + qd * 8 + 4) * 4);
          const u32x4 o = {cvt_pk(lo_f(v.x) * r0[0], hi_f(v.x) * r0[1]), cvt_pk(lo_f(v.y) * r0[2], hi_f(v.y) * r0[3]), cvt_pk(lo_f(v.z) * r1[0], hi_f(v.z) * r1[1]), cvt_pk(lo_f(v.w) * r1[2], hi_f(v.w) * r1[3])};
          __builtin_amdgcn_raw_buffer_store_b128(o, rq, (unsigned)(((144 * jp + c) * S + 32 * sb2 + 8 * qd) * 2), 0, 16); } } }
    typedef Mma<R, 9, 2, true> M;
    M m; m.init(lane, 0, 2 * wid);
    typename M::Frag f0; unsigned so = 0;
    bar();
    m.rd(f0, so, 0);
    auto hook = [&](int it) {
      if (it == 1) wait_vm<0>();
      if (it == 2 && tid == 0) __hip_atomic_fetch_add((GAS unsigned*)(fl + FL_PREP + (w & 7) * 32), 1u, __ATOMIC_RELAXED, __HIP_MEMORY_SCOPE_AGENT);
      if (it == 8 && j == 0 && wid == 0) {
        for (int spin = 0;; spin++) {
          const unsigned v = lane < 8 ? ld_relaxed(fl + FL_PREP + lane * 32) : 32u;
          if (__all(v == 32u)) break;
          if (spin > SPIN_CAP) { if (lane == 0) st_relaxed(fl + FL_TMO, 2u); break; }
          __builtin_amdgcn_s_sleep(1);
        }
      }
    };
    mma_pass<R, true>(m, f0, so, n, hook);
    STAMP(ws, 512 + b, 2);
    if (j == 0) k3_finish<0>(m, wid, lane, i, t, x, b2, out, ws);
    else if (j == 1) k3_finish<1>(m, wid, lane, i, t, x, b2, out, ws);
    else if (j == 2) k3_finish<2>(m, wid, lane, i, t, x, b2, out, ws);
    else k3_finish<3>(m, wid, lane, i, t, x, b2, out, ws);
  }
  STAMP(ws, 512 + b, 1);
}
}

extern "C" void kernel_launch(void* const* d_in, const int* in_sizes, int n_in, void* d_out, int out_size, void* d_ws, size_t ws_size, hipStream_t stream) {
  const u16* x = (const u16*)d_in[0];
  const u16* wqkv = (const u16*)d_in[1];
  const u16* wo = (const u16*)d_in[2];
  const u16* w1 = (const u16*)d_in[4];
  const u16* b1 = (const u16*)d_in[5];
  const u16* w2 = (const u16*)d_in[6];
  const u16* b2 = (const u16*)d_in[7];
  char* ws = (char*)d_ws;
  (void)hipFuncSetAttribute((const void*)k2_mid, hipFuncAttributeMaxDynamicSharedMemorySize, LDS2);
  (void)hipFuncSetAttribute((const void*)k3_out, hipFuncAttributeMaxDynamicSharedMemorySize, LDS3);
  k1_ln<<<256, 256, 0, stream>>>(x, (const char*)wqkv, (const char*)w1, ws);
  k2_mid<<<256, 512, LDS2, stream>>>(wqkv, w1, b1, ws);
  k3_out<<<256, 512, LDS3, stream>>>(x, wo, w2, b2, (u16*)d_out, ws);
}
```

```cpp
#include <hip/hip_runtime.h>
#include <stdint.h>

namespace {
typedef unsigned short u16;
typedef short s16x4 __attribute__((ext_vector_type(4)));
typedef short s16x8 __attribute__((ext_vector_type(8)));
typedef __bf16 bf16x8 __attribute__((ext_vector_type(8)));
typedef float f32x2 __attribute__((ext_vector_type(2)));
typedef float f32x4 __attribute__((ext_vector_type(4)));
typedef unsigned u32x2 __attribute__((ext_vector_type(2)));
typedef unsigned u32x4 __attribute__((ext_vector_type(4)));
#define LAS __attribute__((address_space(3)))
#define GAS __attribute__((address_space(1)))
#define DI __device__ __forceinline__
typedef __amdgpu_buffer_rsrc_t rsrc_t;

constexpr int S = 2048, D = 576, F = 4608;
constexpr unsigned OFF_FLAGS = 0;
constexpr unsigned OFF_HT   = 65536;
constexpr unsigned OFF_XN   = OFF_HT + 2359296;
constexpr unsigned OFF_GX   = OFF_XN + 2359296;
constexpr unsigned GX_BYTES = 648u * 576u * 16u;
constexpr unsigned OFF_PQ   = OFF_GX + GX_BYTES;
constexpr unsigned OFF_PQS  = OFF_PQ + 2359296;
constexpr unsigned OFF_KVS  = OFF_PQS + 2359296;
constexpr unsigned KVS_SLAB = 8 * 18 * 20;
constexpr unsigned OFF_ZS   = OFF_KVS + 64u * KVS_SLAB * 4u;
constexpr unsigned OFF_PART = OFF_ZS + 64u * 144u * 4u + 2048u;
constexpr unsigned PART_BYTES = 64u * 16u * 12288u;
constexpr unsigned FL_PREP = 0;
constexpr unsigned FL_G = 384;
constexpr unsigned FL_QKV = 576;
constexpr unsigned FL_XF = 1024;
constexpr unsigned FL_TMO = 5120;
constexpr int SPIN_CAP = 1 << 18;

#define PF_ROWS 704
#define W2_QST 11
#define KV_WARM 3
#define CODE_PF 1

static_assert(W2_QST + 2 * KV_WARM <= 18 && PF_ROWS <= 1792, "warm-up ranges");
constexpr unsigned K2_CODE = 49152;

DI float bf2f(unsigned b) { return __uint_as_float(b << 16); }
typedef __bf16 bf16x2 __attribute__((ext_vector_type(2)));
DI unsigned cvt_pk(float lo, float hi) { const bf16x2 v = {(__bf16)lo, (__bf16)hi}; return __builtin_bit_cast(unsigned, v); }
DI float lo_f(unsigned p) { return __uint_as_float(p << 16); }
DI float hi_f(unsigned p) { return __uint_as_float(p & 0xffff0000u); }
DI float rbf(float x) { return lo_f(cvt_pk(x, x)); }
DI rsrc_t mk_rsrc(const void* p, unsigned bytes) { return __builtin_amdgcn_make_buffer_rsrc((void*)p, (short)0, (int)bytes, 0x00020000); }
template <int AUX>
DI void dma16(rsrc_t r, unsigned voff, unsigned soff, unsigned ldsaddr) {
  ldsaddr = __builtin_amdgcn_readfirstlane(ldsaddr);
  if (AUX == 2) asm volatile("s_mov_b32 m0, %0\n\ts_nop 0\n\tbuffer_load_dwordx4 %1, %2, %3 offen nt lds" ::"s"(ldsaddr), "v"(voff), "s"(r), "s"(soff) : "memory");
  else if (AUX == 0) asm volatile("s_mov_b32 m0, %0\n\ts_nop 0\n\tbuffer_load_dwordx4 %1, %2, %3 offen lds" ::"s"(ldsaddr), "v"(voff), "s"(r), "s"(soff) : "memory");
  else          asm volatile("s_mov_b32 m0, %0\n\ts_nop 0\n\tbuffer_load_dwordx4 %1, %2, %3 offen sc1 lds" ::"s"(ldsaddr), "v"(voff), "s"(r), "s"(soff) : "memory");
}
DI void dma4(rsrc_t r, unsigned voff, unsigned soff, unsigned ldsaddr) {
  ldsaddr = __builtin_amdgcn_readfirstlane(ldsaddr);
  asm volatile("s_mov_b32 m0, %0\n\ts_nop 0\n\tbuffer_load_dword %1, %2, %3 offen lds" ::"s"(ldsaddr), "v"(voff), "s"(r), "s"(soff) : "memory");
}
template <int N> DI void wait_vm() { asm volatile("s_waitcnt vmcnt(%0)" ::"i"(N) : "memory"); }
DI void wait_lgkm0() { asm volatile("s_waitcnt lgkmcnt(0)" ::: "memory"); }
DI void bar() { asm volatile("" ::: "memory"); __builtin_amdgcn_s_barrier(); asm volatile("" ::: "memory"); }
DI void barl() { wait_lgkm0(); bar(); }
DI unsigned rfl(unsigned x) { return __builtin_amdgcn_readfirstlane(x); }
DI s16x8 lds_rd128(unsigned a) { return *(const LAS s16x8*)(size_t)a; }
DI s16x8 lds_rdtr(unsigned a0, unsigned a1) {
  s16x4 lo = __builtin_amdgcn_ds_read_tr16_b64_v4i16((LAS s16x4*)(size_t)a0);
  s16x4 hi = __builtin_amdgcn_ds_read_tr16_b64_v4i16((LAS s16x4*)(size_t)a1);
  return __builtin_shufflevector(lo, hi, 0, 1, 2, 3, 4, 5, 6, 7);
}
DI f32x4 mfma16(s16x8 a, s16x8 b, f32x4 c) { return __builtin_amdgcn_mfma_f32_16x16x32_bf16(__builtin_bit_cast(bf16x8, a), __builtin_bit_cast(bf16x8, b), c, 0, 0, 0); }
template <class T> DI T lds_ld(unsigned a) { return *(const LAS T*)(size_t)a; }
template <class T> DI void lds_st(unsigned a, T v) { *(LAS T*)(size_t)a = v; }

template <int CTRL> DI float dpp_add(float v) { return v + __int_as_float(__builtin_amdgcn_update_dpp(0, __float_as_int(v), CTRL, 0xf, 0xf, true)); }
DI float half_sum(float v) {
  v = dpp_add<0xB1>(v); v = dpp_add<0x4E>(v); v = dpp_add<0x141>(v); v = dpp_add<0x140>(v);
  auto r = __builtin_amdgcn_permlane16_swap(__float_as_uint(v), __float_as_uint(v), false, false);
  return __uint_as_float(r[0]) + __uint_as_float(r[1]);
}
DI void ln_row(float (&v)[18]) {
  float s = 0.f;
#pragma unroll
  for (int i = 0; i < 18; i++) s += v[i];
  const float m = half_sum(s) * (1.0f / 576.0f);
  float q = 0.f;
#pragma unroll
  for (int i = 0; i < 18; i++) { float d = v[i] - m; q += d * d; }
  const float var = half_sum(q) * (1.0f / 576.0f);
  const float mb = rbf(m), den = rbf(sqrtf(rbf(rbf(var) + 1e-5f)));
  const float inv = 1.0f / den;
#pragma unroll
  for (int i = 0; i < 18; i += 2) {
    unsigned p = cvt_pk(v[i] - mb, v[i + 1] - mb);
    p = cvt_pk(lo_f(p) * inv, hi_f(p) * inv);
    v[i] = lo_f(p); v[i + 1] = hi_f(p);
  }
}

}
extern "C" __global__ __launch_bounds__(256) void psum_k1_ln(const u16* __restrict__ x, const char* __restrict__ wqkv, const char* __restrict__ wo, const char* __restrict__ w1, char* __restrict__ ws) {
  __shared__ __attribute__((aligned(16))) char sm[2 * 8 * 1168];
  asm volatile("" ::"s"(x), "s"(wqkv), "s"(wo), "s"(w1), "s"(ws));
  const int tid = threadIdx.x, lane = tid & 63, v = tid >> 6, hh = lane >> 5, l = lane & 31;
  const int b = blockIdx.x, xcd = b & 7, r = b >> 3, w = xcd * 32 + r, kb = w >> 2, g = w & 3;
  const int j = 2 * v + hh, row = 32 * kb + 4 * g + (j & 3) + 16 * (j >> 2);
  if (tid < 32) ((unsigned*)(ws + OFF_FLAGS))[w * 32 + tid] = 0u;
  unsigned xr[9];
  { const rsrc_t rx = mk_rsrc(x, S * D * 2); const unsigned xo = (unsigned)(row * (D * 2) + l * 4);
#pragma unroll
    for (int i = 0; i < 9; i++) asm volatile("buffer_load_dword %0, %1, %2, 0 offen offset:%3" : "=&v"(xr[i]) : "v"(xo), "s"(rx), "i"(128 * i) : "memory"); }
  unsigned pf[4];
  { const int gw = r * 4 + v, l9 = lane < 63 ? lane : 62, rr = l9 / 9, c = l9 - 9 * rr;
#pragma unroll
    for (int k = 0; k < 2; k++) { const int rw = (gw + 128 * k) * 7 + rr; const char* p = w1 + (size_t)(rw < PF_ROWS ? rw : 0) * 9216 + xcd * 1152 + c * 128;
      asm volatile("global_load_dword %0, %1, off" : "=&v"(pf[k]) : "v"(p) : "memory"); }
    const unsigned o = (unsigned)((xcd * 128 + gw) * 64 + lane) * 128u;
    { const char* p = o < 576u * 3456u ? wqkv + o : wo + min(o - 576u * 3456u, 576u * 1152u - 128u);
      asm volatile("global_load_dword %0, %1, off" : "=&v"(pf[2]) : "v"(p) : "memory"); }
    unsigned long long pc; int d1, d2;
    asm volatile("s_getpc_b64 %0\n.Lpsum_anchor%=:\n\ts_mov_b32 %1, psum_k1_ln-.Lpsum_anchor%=\n\ts_mov_b32 %2, psum_k2-.Lpsum_anchor%=" : "=s"(pc), "=s"(d1), "=s"(d2));
    const unsigned lim = !CODE_PF ? 0u : gw < 6 ? K2_CODE : gw < 8 ? 4096u : 0u, oc = (unsigned)((gw < 6 ? gw : 0) * 8192 + lane * 128);
    const char* p = (const char*)(pc + (long long)(gw < 6 ? d2 : d1) + (oc < lim ? oc : 0u));
    asm volatile("global_load_dword %0, %1, off" : "=&v"(pf[3]) : "v"(p) : "memory"); }
  asm volatile("s_waitcnt vmcnt(4)" : "+v"(xr[0]), "+v"(xr[1]), "+v"(xr[2]), "+v"(xr[3]), "+v"(xr[4]), "+v"(xr[5]), "+v"(xr[6]), "+v"(xr[7]), "+v"(xr[8]) :: "memory");
  float f[18];
#pragma unroll
  for (int i = 0; i < 9; i++) { f[2 * i] = lo_f(xr[i]); f[2 * i + 1] = hi_f(xr[i]); }
  const unsigned xs = (unsigned)(size_t)(LAS char*)sm, hs = xs + 8 * 1168;
  ln_row(f);
#pragma unroll
  for (int i = 0; i < 9; i++) lds_st<unsigned>(xs + j * 1168 + (32 * i + l) * 4, (__float_as_uint(f[2 * i]) >> 16) | (__float_as_uint(f[2 * i + 1]) & 0xffff0000u));
  ln_row(f);
#pragma unroll
  for (int i = 0; i < 9; i++) lds_st<unsigned>(hs + j * 1168 + (32 * i + l) * 4, (__float_as_uint(f[2 * i]) >> 16) | (__float_as_uint(f[2 * i + 1]) & 0xffff0000u));
  __syncthreads();
#pragma unroll
  for (int m = 0; m < 2; m++) {
    const int dp = tid + 256 * m;
    if (dp < 288) {
      unsigned a[8];
#pragma unroll
      for (int jj = 0; jj < 8; jj++) a[jj] = lds_ld<unsigned>(hs + jj * 1168 + dp * 4);
      u32x4 c0, c1;
#pragma unroll
      for (int k = 0; k < 4; k++) { c0[k] = __builtin_amdgcn_perm(a[2 * k + 1], a[2 * k], 0x05040100u); c1[k] = __builtin_amdgcn_perm(a[2 * k + 1], a[2 * k], 0x07060302u); }
      u32x4* o = (u32x4*)(ws + OFF_HT + ((size_t)(kb * 4 + g) * D + 2 * dp) * 16);
      o[0] = c0; o[1] = c1;
    }
  }
#pragma unroll
  for (int m = 0; m < 3; m++) {
    const int idx = tid + 256 * m;
    if (idx < 576) {
      const int qc = idx >> 3, jj = idx & 7, d0 = 32 * (qc >> 2) + 4 * (qc & 3);
      u32x2 a = lds_ld<u32x2>(xs + jj * 1168 + d0 * 2), c = lds_ld<u32x2>(xs + jj * 1168 + d0 * 2 + 32);
      const int rw = 32 * kb + 4 * g + (jj & 3) + 16 * (jj >> 2);
      *(u32x4*)(ws + OFF_XN + ((size_t)qc * S + rw) * 16) = (u32x4){a.x, a.y, c.x, c.y};
    }
  }
  asm volatile("s_waitcnt vmcnt(0)" ::"v"(pf[0]), "v"(pf[1]), "v"(pf[2]), "v"(pf[3]) : "memory");
}
namespace {

template <int XR_, int WRB_, int NS_> struct Ring {
  static constexpr int XR = XR_, WRB = WRB_, NS = NS_;
  static constexpr int XBYTES = 8 * XR * 16, WBYTES = 64 * WRB, STG = XBYTES + WBYTES;
  static constexpr int XP = XBYTES / 1024, WP = WBYTES / 1024;
  static constexpr int XQ = (XP + 3) / 4, WQ = WP / 4;
  static constexpr int BYTES = NS * STG;
};
typedef Ring<144, 256, 4> RingA;
typedef Ring<128, 384, 3> RingB;

template <class R> struct LoadOff { unsigned x[R::XQ]; unsigned w[R::WQ]; };
template <class R> DI void load_off(LoadOff<R>& o, int lw, int lane, int xld  , int x0, int wld  , int w0  ) {
#pragma unroll
  for (int q = 0; q < R::XQ; q++) { int idx = 64 * (lw + 4 * q) + lane; int cr = idx / R::XR, m = idx - cr * R::XR; o.x[q] = (unsigned)((cr * xld + x0 + m) * 16); }
#pragma unroll
  for (int q = 0; q < R::WQ; q++) {
    int idx = 64 * (lw + 4 * q) + lane;
    if (R::WRB == 256) { int row = idx >> 4, c = (idx & 15) ^ ((row & 7) << 1); o.w[q] = (unsigned)(row * wld + w0 + c * 16); }
    else { int row = idx / 24, cp = idx - row * 24, c = cp ^ (((row >> 1) & 3) << 1); o.w[q] = (unsigned)(row * wld + w0 + c * 16); }
  }
}
template <class R, int AUX, class Src>
DI void ring_issue_w(const LoadOff<R>& o, const Src& src, int lw, int s, int slot) {
  rsrc_t rx, rw; unsigned sx, sw; bool wsc1;
  src.get(s, rx, sx, rw, sw, wsc1);
  const unsigned lb = (unsigned)slot * R::STG + lw * 1024u + R::XBYTES;
  if (AUX && wsc1) {
#pragma unroll
    for (int q = 0; q < R::WQ; q++) dma16<1>(rw, o.w[q], sw, lb + q * 4096u);
  } else {
#pragma unroll
    for (int q = 0; q < R::WQ; q++) dma16<0>(rw, o.w[q], sw, lb + q * 4096u);
  }
}
template <class R, int NX, int AUX, class Src>
DI void ring_issue_x(const LoadOff<R>& o, const Src& src, int lw, int s, int slot) {
  rsrc_t rx, rw; unsigned sx, sw; bool wsc1;
  src.get(s, rx, sx, rw, sw, wsc1);
  const unsigned lb = (unsigned)slot * R::STG + lw * 1024u;
#pragma unroll
  for (int q = 0; q < NX; q++) dma16<AUX>(rx, o.x[q], sx, lb + q * 4096u);
}
template <class R, class Src>
DI void ring_prologue_w(const LoadOff<R>& o, const Src& src, int lw) {
#pragma unroll
  for (int s = 0; s < R::NS - 1; s++) ring_issue_w<R, 1>(o, src, lw, s, s);
}
template <class R, int NX, int AUX, class Src, class Tail>
DI void ring_run_n(const LoadOff<R>& o, const Src& src, int lw, int n, const Tail& tail) {
  constexpr int P = NX + R::WQ, NS = R::NS;
  int slot = NS - 1, i0 = 0;
  auto step = [&](int i) { bar(); ring_issue_x<R, NX, AUX>(o, src, lw, i + NS - 1, slot); ring_issue_w<R, AUX>(o, src, lw, i + NS - 1, slot); slot = (slot + 1 == NS) ? 0 : slot + 1; };
  if (AUX) {
#pragma unroll
    for (int s = 0; s < NS - 1; s++) ring_issue_x<R, NX, AUX>(o, src, lw, s, s);
    wait_vm<NX*(NS - 2)>(); step(0);
    if (NS == 4) { wait_vm<NX + P>(); step(1); }
    i0 = NS - 2;
  } else {
#pragma unroll
    for (int s = 0; s < NS - 1; s++) { ring_issue_x<R, NX, AUX>(o, src, lw, s, s); ring_issue_w<R, AUX>(o, src, lw, s, s); }
  }
  for (int i = i0; i < n - (NS - 1); i++) { wait_vm<P*(NS - 2)>(); step(i); }
  if (NS >= 4) { wait_vm<P * ((NS >= 4) ? 2 : 0)>(); bar(); }
  wait_vm<P>(); bar();
  tail((unsigned)(((n - 3) % NS) * R::STG));
  wait_vm<Tail::N>(); bar();
}
struct NoTail { static constexpr int N = 0; DI void operator()(unsigned) const {} };
template <class R, int AUX, class Src, class Tail>
DI void ring_run(const LoadOff<R>& o, const Src& src, int lw, int n, const Tail& tail) {
  if (R::XP % 4 == 0 || lw < R::XP % 4) ring_run_n<R, R::XQ, AUX>(o, src, lw, n, tail);
  else ring_run_n<R, R::XQ - 1, AUX>(o, src, lw, n, tail);
}

template <class R, int XB, int WB, bool XA> struct Mma {
  f32x4 acc[XB][WB];
  unsigned xa;
  static constexpr int NWA = (R::WRB == 256) ? WB : (WB < 4 ? WB : 4);
  unsigned wa[NWA];
  DI void init(int lane, int xb0, int wb0) {
    const int g = lane >> 4, l15 = lane & 15, q = l15 >> 2, p = lane & 3;
    xa = (unsigned)((g * R::XR + xb0 * 16 + l15) * 16);
    const unsigned wrow = (unsigned)(R::XBYTES + (4 * g + q) * R::WRB + p * 8);
    if (R::WRB == 256) {
      const int r7 = (4 * g + q) & 7;
#pragma unroll
      for (int u = 0; u < NWA; u++) wa[u] = wrow + (((wb0 + u) ^ r7) * 32);
    } else {
      const int f = ((4 * g + q) >> 1) & 3;
#pragma unroll
      for (int u = 0; u < NWA; u++) { const int cb = wb0 + u; wa[u] = wrow + (cb >> 2) * 128 + (((cb & 3) ^ f) * 32); }
    }
    zero();
  }
  DI void zero() {
#pragma unroll
    for (int x = 0; x < XB; x++)
#pragma unroll
      for (int u = 0; u < WB; u++) acc[x][u] = (f32x4){0.f, 0.f, 0.f, 0.f};
  }
  struct Frag { s16x8 x[XB]; s16x8 w[WB]; };
  struct Addr { unsigned x; unsigned w[NWA]; };
  Addr cur;
  DI Addr addr(unsigned so) const { Addr a; a.x = xa + so;
#pragma unroll
    for (int u = 0; u < NWA; u++) a.w[u] = wa[u] + so;
    return a; }
  DI void rd(Frag& fr, const Addr& ad, int kk) const {
#pragma unroll
    for (int u = 0; u < WB; u++) {
      const unsigned a = (R::WRB == 256 ? ad.w[u] : ad.w[u & 3] + (u >> 2) * 128) + kk * 32 * R::WRB;
      fr.w[u] = lds_rdtr(a, a + 16 * R::WRB);
    }
#pragma unroll
    for (int x = 0; x < XB; x++) fr.x[x] = lds_rd128(ad.x + kk * (4 * R::XR * 16) + x * 256);
  }
  DI void mm(const Frag& fr) {
#pragma unroll
    for (int x = 0; x < XB; x++)
#pragma unroll
      for (int u = 0; u < WB; u++) acc[x][u] = XA ? mfma16(fr.x[x], fr.w[u], acc[x][u]) : mfma16(fr.w[u], fr.x[x], acc[x][u]);
  }
  static DI void ilv() {
    constexpr int NR = XB + 2 * WB, NM = XB * WB, NI = NR < NM ? NR : NM;
#pragma unroll
    for (int z = 0; z < NI; z++) {
      __builtin_amdgcn_sched_group_barrier(0x8, 1, 0); __builtin_amdgcn_sched_group_barrier(0x100, 1, 0);
      if (NR > NM && z < NR - NM) __builtin_amdgcn_sched_group_barrier(0x100, 1, 0);
    }
    if constexpr (NM > NI) __builtin_amdgcn_sched_group_barrier(0x8, NM - NI, 0);
  }
};
template <class R, bool LAST, class M>
DI void mma_pass(M& m, typename M::Frag& f0, unsigned& so, int n) {
  typename M::Frag f1;
  const int nfull = LAST ? n - 1 : n;
#pragma unroll 1
  for (int i = 0; i < nfull; i++) {
    unsigned sn = so + R::STG; if (sn == R::NS * R::STG) sn = 0;
    typename M::Addr an = m.addr(sn);
    m.rd(f1, m.cur, 1);
    m.mm(f0);
    M::ilv();
    __builtin_amdgcn_sched_barrier(0);
    wait_lgkm0();
    bar();
    __builtin_amdgcn_sched_barrier(0);
    so = sn; m.cur = an;
    m.rd(f0, m.cur, 0);
    m.mm(f1);
    M::ilv();
    __builtin_amdgcn_sched_barrier(0);
  }
  if (LAST) { m.rd(f1, m.cur, 1); m.mm(f0); M::ilv(); __builtin_amdgcn_sched_barrier(0); m.mm(f1); }
}

template <class Map>
DI void warm(const char* base, int u0, int u1, int gw, int nw, int lane, const Map& map) {
  for (int u = u0 + gw; u < u1; u += 2 * nw) {
    const unsigned a = *(const unsigned*)(base + map(u, lane));
    const unsigned b = *(const unsigned*)(base + map(u + nw < u1 ? u + nw : u, lane));
    asm volatile("" ::"v"(a), "v"(b));
  }
}
struct MapW1 { int xcd;
  DI size_t operator()(int u, int lane) const { const int l = lane < 63 ? lane : 62, r = l / 9, c = l - 9 * r; int row = PF_ROWS + u * 7 + r; row = row < 2048 ? row : 2047;
    return (size_t)row * 9216 + xcd * 1152 + c * 128; } };
constexpr int W1_UNITS = (2048 - PF_ROWS + 6) / 7;
struct MapW2 { int xcd;
  DI size_t operator()(int u, int lane) const {
    const int j = xcd >> 1, s = u >> 4, st = 18 * j + s;
    return (size_t)(st * 64 + (u & 15) * 4 + (lane >> 4)) * 4096 + (xcd & 1) * 2048 + (lane & 15) * 128; } };
DI unsigned ld_relaxed(const unsigned* p) { return __hip_atomic_load((const GAS unsigned*)p, __ATOMIC_RELAXED, __HIP_MEMORY_SCOPE_AGENT); }
DI void st_relaxed(unsigned* p, unsigned v) { __hip_atomic_store((GAS unsigned*)p, v, __ATOMIC_RELAXED, __HIP_MEMORY_SCOPE_AGENT); }
DI void add_relaxed(unsigned* p) { (void)__hip_atomic_fetch_add((GAS unsigned*)p, 1u, __ATOMIC_RELAXED, __HIP_MEMORY_SCOPE_AGENT); }
DI void poll_words(const unsigned* p, int stride, unsigned lanemask_lo, unsigned want, unsigned* tmo, unsigned code, int lane) {
  const bool on = lane < 32 && ((lanemask_lo >> lane) & 1u);
  for (int spin = 0;; spin++) {
    const unsigned v = on ? ld_relaxed(p + lane * stride) : want;
    if (__all(v == want)) break;
    if (spin > SPIN_CAP) { if (lane == 0) st_relaxed(tmo, code); break; }
    __builtin_amdgcn_s_sleep(1);
  }
}

DI void quad_barrier(unsigned ctr, unsigned round, int lane) {
  wait_lgkm0();
  if (lane == 0) __hip_atomic_fetch_add((LAS unsigned*)(size_t)ctr, 1u, __ATOMIC_RELAXED, __HIP_MEMORY_SCOPE_WORKGROUP);
  while (rfl(*(volatile LAS unsigned*)(size_t)ctr) < 4u * round) __builtin_amdgcn_s_sleep(1);
  asm volatile("" ::: "memory");
}

DI void lds_wait(unsigned ctr, unsigned want) { while (rfl(*(volatile LAS unsigned*)(size_t)ctr) < want) __builtin_amdgcn_s_sleep(2); asm volatile("" ::: "memory"); }
DI void lds_signal(unsigned ctr, int lane) { if (lane == 0) __hip_atomic_fetch_add((LAS unsigned*)(size_t)ctr, 1u, __ATOMIC_RELAXED, __HIP_MEMORY_SCOPE_WORKGROUP); }
DI void st16_wt(rsrc_t r, unsigned voff, unsigned soff, u32x4 v) { __builtin_amdgcn_raw_buffer_store_b128(v, r, voff, soff, 16); }
DI void st8_wt(rsrc_t r, unsigned voff, unsigned soff, u32x2 v) { __builtin_amdgcn_raw_buffer_store_b64(v, r, voff, soff, 16); }
DI void st4_wt(rsrc_t r, unsigned voff, unsigned soff, unsigned v) { __builtin_amdgcn_raw_buffer_store_b32(v, r, voff, soff, 16); }
DI u32x4 ld16_sc1(rsrc_t r, unsigned voff, unsigned soff) { return __builtin_amdgcn_raw_buffer_load_b128(r, voff, soff, 16); }
DI unsigned ld4_sc1(rsrc_t r, unsigned voff, unsigned soff) { return __builtin_amdgcn_raw_buffer_load_b32(r, voff, soff, 16); }

struct SrcLin { rsrc_t rx, rw; unsigned xstep, wstep;
  DI void get(int s, rsrc_t& a, unsigned& sx, rsrc_t& b, unsigned& sw, bool& sc1) const { a = rx; b = rw; sx = s * xstep; sw = s * wstep; sc1 = false; } };
struct SrcKV { rsrc_t rx, rw;
  DI void get(int s, rsrc_t& a, unsigned& sx, rsrc_t& b, unsigned& sw, bool& sc1) const {
    const int p = s >= 9, k = s - 9 * p; a = rx; b = rw; sx = k * (8 * S * 16); sw = k * (64 * 3456) + p * 1152; sc1 = false; } };

DI unsigned phi_pk(float a, float b) {
  const f32x2 t = {a, b}, u = t * 1.4426950408889634f;
  const f32x2 e = (f32x2){__builtin_amdgcn_fmed3f(__builtin_amdgcn_exp2f(u.x), 0.f, 1.f), __builtin_amdgcn_fmed3f(__builtin_amdgcn_exp2f(u.y), 0.f, 1.f)} + 1e-6f;
  const f32x2 r = (f32x2){fmaxf(a, 0.f), fmaxf(b, 0.f)} + e;
  return cvt_pk(r.x, r.y);
}
DI unsigned gelu_pk(f32x2 v) {
  constexpr float c0 = -2.0f * 0.7978845608028654f * 1.4426950408889634f, c1 = c0 * 0.044715f;
  const f32x2 w = (v * v * c1 + c0) * v;
  const f32x2 d = (f32x2){__builtin_amdgcn_exp2f(w.x), __builtin_amdgcn_exp2f(w.y)} + 1.0f;
  const f32x2 gl = v * (f32x2){__builtin_amdgcn_rcpf(d.x), __builtin_amdgcn_rcpf(d.y)};
  return cvt_pk(gl.x, gl.y);
}

constexpr int NG0 = 18;
#define GFIRST 3
static_assert(2 * GFIRST - 3 >= 3, "the late G check sits in front of barrier 3");
struct SrcG2 { rsrc_t rx, rw, rp; int j;
  DI void get(int s, rsrc_t& a, unsigned& sx, rsrc_t& b, unsigned& sw, bool& sc1) const {
    const int st = s < NG0 ? NG0 * j + s : 72 + (j == 0 ? 0 : 2 * j + 1) + (s - NG0);
    a = rx; sx = st * (8 * D * 16); sc1 = st >= 72;
    if (st >= 72) { b = rp; sw = (st - 72) * (64 * S * 2); } else { b = rw; sw = st * (64 * S * 2); } } };

constexpr unsigned KOFF = RingB::BYTES;
constexpr unsigned PL_PQ = RingA::BYTES, PL_Z = PL_PQ + 9216, PL_RD = PL_Z + 576, PL_KV = PL_RD + 1024;
constexpr unsigned CTR_C = KOFF + 9 * 4096, CTR_L = CTR_C + 16, CTR_S = CTR_C + 32, LDS_SINK = CTR_C + 64, LDS_ALL = LDS_SINK + 256;
static_assert(PL_KV + 4096 <= CTR_C && LDS_ALL <= 163840, "lds");

struct Args { const u16 *x, *wqkv, *wo, *w1, *b1, *w2, *b2; u16* out; char* ws; };

struct P2 { int i, t, j, n; };
DI P2 p2_of(int b) { const int xcd = b & 7, r = b >> 3, j = xcd >> 1; return P2{8 * (xcd & 1) + (r >> 2), r & 3, j, j == 0 ? 21 : 20}; }
struct ResidTail { static constexpr int N = 4; const u16 *x, *b2; int lw, lane, i, t, j;
  DI void operator()(unsigned slot) const {
    const rsrc_t rx = mk_rsrc(x, S * D * 2), rb = mk_rsrc(b2, S * 2);
#pragma unroll
    for (int q = 0; q < 3; q++) { const int idx = 64 * (lw + 4 * q) + lane, row = idx / 6, c = idx - 6 * row;
      dma16<0>(rx, (unsigned)((i * 128 + row) * (D * 2) + (t * 144 + 32 * j) * 2 + c * 16), 0u, slot + (lw + 4 * q) * 1024u); }
    dma4(rb, (unsigned)(i * 256 + lane * 4), 0u, slot + 12288u + lw * 256u);
  } };
DI void p2_load(const Args& a, int lw, int lane, int b, bool after_g) {
  typedef RingA R;
  char* ws = a.ws; unsigned* fl = (unsigned*)(ws + OFF_FLAGS);
  const P2 p = p2_of(b);
  LoadOff<R> o; load_off<R>(o, lw, lane, D, p.t * 144, S * 2, p.i * 256);
  const SrcG2 src{mk_rsrc(ws + OFF_GX, GX_BYTES), mk_rsrc(a.w2, (unsigned)F * S * 2), mk_rsrc(ws + OFF_PQS, S * D * 2), p.j};
  if (after_g) lds_wait(CTR_S, 4u);
  ring_prologue_w<R>(o, src, lw);
  if (!after_g && (b >> 3) < 26) {
    const rsrc_t rw = mk_rsrc(a.w2, (unsigned)F * S * 2);
#pragma unroll
    for (int k = 0; k < KV_WARM; k++) dma4(rw, (unsigned)MapW2{b & 7}(W2_QST * 16 + k * 32 + ((b >> 3) - 18) * 4 + lw, lane), 0u, LDS_SINK);
  }
  if (p.j > 0) {
    const int ua = (p.j - 1) * 64 + p.i * 4 + p.t, hp = ua / 12, dq = ua - 12 * hp, tq = lw * 64 + lane, g = lane >> 4, l15 = lane & 15;
    unsigned wv[2][6];
#pragma unroll
    for (int hh = 0; hh < 2; hh++) {
      const u16* wp = a.wo + (size_t)(18 * (2 * hp + hh)) * D + 48 * dq + 16 * (lw < 3 ? lw : 0) + l15;
#pragma unroll
      for (int z = 0; z < 4; z++) wv[hh][z] = wp[(4 * g + z) * D];
      wv[hh][4] = wp[16 * D]; wv[hh][5] = wp[17 * D];
    }
    lds_st<u32x4>(PL_KV + tq * 16, (u32x4){0u, 0u, 0u, 0u});
    poll_words(fl + FL_QKV + (hp >> 2), 4, 0xffffu, 1u, fl + FL_TMO, 3u, lane);
    const int t2 = tq < 180 ? tq : 0, h2 = t2 >= 90, rem = t2 - 90 * h2, dl = rem / 5, cq = rem - 5 * dl;
    const rsrc_t rk = mk_rsrc(ws + OFF_KVS, 64u * KVS_SLAB * 4u);
    u32x4 kv[16];
#pragma unroll
    for (int s = 0; s < 16; s++) kv[s] = ld16_sc1(rk, (unsigned)(((hp >> 2) * 8 + ((2 * hp + h2) & 7)) * 1440 + rem * 16), (unsigned)(s * 4 * KVS_SLAB * 4));
    quad_barrier(CTR_L, 1, lane);
    f32x4 v = {0.f, 0.f, 0.f, 0.f};
#pragma unroll
    for (int s = 0; s < 16; s++) v += __builtin_bit_cast(f32x4, kv[s]);
    if (tq < 180) { const unsigned la = PL_KV + (h2 * 32 + dl) * 64;
      if (cq < 4) lds_st<u32x2>(la + cq * 16, (u32x2){cvt_pk(v[0], v[1]), cvt_pk(v[2], v[3])});
      else lds_st<unsigned>(la + 8, cvt_pk(v[0], v[1])); }
    quad_barrier(CTR_L, 2, lane);
    if (lw < 3) {
      s16x8 af[2][2], bf[2];
#pragma unroll
      for (int hh = 0; hh < 2; hh++) {
        bf[hh] = __builtin_bit_cast(s16x8, (u32x4){wv[hh][0] | (wv[hh][1] << 16), wv[hh][2] | (wv[hh][3] << 16), g == 0 ? wv[hh][4] | (wv[hh][5] << 16) : 0u, 0u});
#pragma unroll
        for (int cb = 0; cb < 2; cb++) af[hh][cb] = lds_rd128(PL_KV + (hh * 32 + 16 * cb + l15) * 64 + g * 16);
      }
      const f32x4 zero = {0.f, 0.f, 0.f, 0.f};
      const f32x4 a0 = mfma16(af[0][0], bf[0], zero), a1 = mfma16(af[0][1], bf[0], zero), b0 = mfma16(af[1][0], bf[1], zero), b1v = mfma16(af[1][1], bf[1], zero);
      const rsrc_t rg = mk_rsrc(ws + OFF_GX, GX_BYTES);
      const unsigned vo = (unsigned)((F / 8) * (D * 16) + (48 * dq + 16 * lw + l15) * 16);
      st16_wt(rg, vo + (hp * 4 + g) * (D * 16), 0, (u32x4){cvt_pk(a0[0], a0[1]), cvt_pk(a0[2], a0[3]), cvt_pk(b0[0], b0[1]), cvt_pk(b0[2], b0[3])});
      const int r5 = (4 * hp) & 31;
      if (g == 0) st8_wt(rg, vo + ((16 + (hp >> 3)) * 4 + ((r5 & 15) >> 2)) * (D * 16) + (r5 >> 4) * 8, 0, (u32x2){cvt_pk(a1[0], a1[1]), cvt_pk(b1v[0], b1v[1])});
    }
  }
  bar();
  const ResidTail rt{a.x, a.b2, lw, lane, p.i, p.t, p.j};
  ring_run<R, 1>(o, src, lw, p.n, rt);
  wait_vm<0>();
  bar();
}

DI constexpr int own_of(int x) { return x < 2 ? 0 : x < 4 ? 1 : x < 6 ? 2 : 3; }
template <int J, class M>
DI void p2_finish(M& m, unsigned rslot, int wid, int lane, int i, int t, u16* out, char* ws) {
  const int T = i * 4 + t, g = lane >> 4, l15 = lane & 15;
  unsigned* fl = (unsigned*)(ws + OFF_FLAGS);
  const rsrc_t rp = mk_rsrc(ws + OFF_PART, PART_BYTES);
  const unsigned vo = (unsigned)(wid * 3 * 1024 + lane * 16);
#pragma unroll
  for (int b = 0; b < 9; b++) {
    const int dst = own_of(b);
    if (dst != J) {
      const f32x4 a = m.acc[b][0], c = m.acc[b][1];
      st16_wt(rp, vo + (b - 2 * dst) * 1024, (unsigned)(((T * 4 + dst) * 4 + J) * 12288), (u32x4){cvt_pk(a[0], a[1]), cvt_pk(a[2], a[3]), cvt_pk(c[0], c[1]), cvt_pk(c[2], c[3])});
    }
  }
  constexpr int LO = 2 * J, N = J < 3 ? 2 : 3;
  wait_vm<0>();
  bar();
  if (wid == 0) {
    if (lane == 0) st_relaxed(fl + FL_XF + (T * 4 + J) * 16, 1u);
    poll_words(fl + FL_XF + T * 64, 16, 0xfu & ~(1u << J), 1u, fl + FL_TMO, 1u, lane);
  }
  bar();
  u32x4 p[3][N];
#pragma unroll
  for (int sr = 0, z = 0; sr < 4; sr++) if (sr != J) {
#pragma unroll
    for (int k = 0; k < N; k++) p[z][k] = ld16_sc1(rp, vo + k * 1024, (unsigned)(((T * 4 + J) * 4 + sr) * 12288));
    z++;
  }
#pragma unroll
  for (int k = 0; k < N; k++)
#pragma unroll
    for (int u = 0; u < 2; u++) {
      f32x4 s = {0.f, 0.f, 0.f, 0.f};
#pragma unroll
      for (int sr = 0, z = 0; sr < 4; sr++) {
        if (sr == J) s += m.acc[LO + k][u];
        else { const unsigned a = p[z][k][2 * u], c = p[z][k][2 * u + 1]; s += (f32x4){lo_f(a), hi_f(a), lo_f(c), hi_f(c)}; z++; }
      }
      const unsigned rrow = 32 * wid + 16 * u + l15;
      const u32x2 xv = lds_ld<u32x2>(rslot + rrow * 96 + k * 32 + g * 8);
      const float bb = bf2f(lds_ld<u16>(rslot + 12288 + rrow * 2));
      *(u32x2*)(out + (size_t)(i * 128 + 32 * wid + 16 * u + l15) * D + t * 144 + 16 * (LO + k) + 4 * g) =
          (u32x2){cvt_pk(lo_f(xv.x) + s[0] + bb, hi_f(xv.x) + s[1] + bb), cvt_pk(lo_f(xv.y) + s[2] + bb, hi_f(xv.y) + s[3] + bb)};
    }
}
struct Pqs {
  int w, jp, sb2, wid, lane; char* ws; u32x4 pq[3]; unsigned zv[16];
  DI void init(const Args& a, int wid_, int lane_, int b) { w = (b & 7) * 32 + (b >> 3); jp = w & 3; sb2 = w >> 2; wid = wid_; lane = lane_; ws = a.ws; }
  DI const unsigned* flag_ptr() const {
    return (const unsigned*)(ws + OFF_FLAGS) + (lane < 16 ? FL_QKV + jp + 4 * lane : FL_QKV + 64 + 3 * (sb2 >> 2) + (lane < 19 ? lane - 16 : 0)); }
  DI void poll() const {
    const unsigned* p = flag_ptr();
    for (int spin = 0;; spin++) {
      if (__all(ld_relaxed(p) == 1u)) break;
      if (spin > SPIN_CAP) { if (lane == 0) st_relaxed((unsigned*)(ws + OFF_FLAGS) + FL_TMO, 4u); break; }
      __builtin_amdgcn_s_sleep(2);
    } }
  static constexpr int NLOADS = 19;
  DI void issue() {
    const rsrc_t rq = mk_rsrc(ws + OFF_PQ, S * D * 2), rz = mk_rsrc(ws + OFF_ZS, 64u * 144u * 4u);
#pragma unroll
    for (int m3 = 0; m3 < 3; m3++) { const int idx = lane + 64 * m3 < 144 ? lane + 64 * m3 : lane; const int mm = idx >= 72, rem = idx - mm * 72;
      const unsigned vo = (unsigned)(((2 * sb2 + mm) * D + 144 * jp + 36 * wid) * 32 + rem * 16);
      asm volatile("buffer_load_dwordx4 %0, %1, %2, 0 offen sc1" : "=&v"(pq[m3]) : "v"(vo), "s"(rq) : "memory"); }
    const unsigned zo = (unsigned)((jp * 144 + 36 * wid + (lane < 36 ? lane : 0)) * 4);
#pragma unroll
    for (int s = 0; s < 16; s++) asm volatile("buffer_load_dword %0, %1, %2, %3 offen sc1" : "=&v"(zv[s]) : "v"(zo), "s"(rz), "s"((unsigned)(s * 4 * 144 * 4)) : "memory");
  }
  DI void finish() { wait(); compute(); }
  DI void wait() {
    asm volatile("s_waitcnt vmcnt(0)"
                 : "+v"(pq[0]), "+v"(pq[1]), "+v"(pq[2]), "+v"(zv[0]), "+v"(zv[1]), "+v"(zv[2]), "+v"(zv[3]), "+v"(zv[4]), "+v"(zv[5]), "+v"(zv[6]), "+v"(zv[7]),
                   "+v"(zv[8]), "+v"(zv[9]), "+v"(zv[10]), "+v"(zv[11]), "+v"(zv[12]), "+v"(zv[13]), "+v"(zv[14]), "+v"(zv[15])
                 :: "memory");
  }
  DI void compute() {
    const unsigned lp = PL_PQ + wid * 2304, lz = PL_Z + wid * 144, lr = PL_RD + wid * 256;
    const rsrc_t rs = mk_rsrc(ws + OFF_PQS, S * D * 2);
#pragma unroll
    for (int m3 = 0; m3 < 3; m3++) { const int idx = lane + 64 * m3; if (idx < 144) { const int mm = idx >= 72, rem = idx - mm * 72; lds_st<u32x4>(lp + (rem >> 1) * 64 + mm * 32 + (rem & 1) * 16, pq[m3]); } }
    { float zs = 0.f;
#pragma unroll
      for (int s = 0; s < 16; s++) zs += __uint_as_float(zv[s]);
      if (lane < 36) lds_st<float>(lz + lane * 4, rbf(zs)); }
    wait_lgkm0();
    { const int hh = lane >> 5, s = lane & 31; float den = 0.f;
      unsigned pv[18]; float zz[18];
#pragma unroll
      for (int dd = 0; dd < 18; dd++) { pv[dd] = lds_ld<u16>(lp + (18 * hh + dd) * 64 + s * 2); zz[dd] = lds_ld<float>(lz + (18 * hh + dd) * 4); }
#pragma unroll
      for (int dd = 0; dd < 18; dd++) den += bf2f(pv[dd]) * zz[dd];
      lds_st<float>(lr + (hh * 32 + s) * 4, __builtin_amdgcn_rcpf(rbf(rbf(den) + 1e-6f))); }
    wait_lgkm0();
#pragma unroll
    for (int m3 = 0; m3 < 3; m3++) { const int idx = lane + 64 * m3;
      if (idx < 144) { const int c = idx >> 2, qd = idx & 3, hh = c >= 18;
        const u32x4 v = lds_ld<u32x4>(lp + c * 64 + qd * 16);
        const f32x4 r0 = lds_ld<f32x4>(lr + (hh * 32 + qd * 8) * 4), r1 = lds_ld<f32x4>(lr + (hh * 32 + qd * 8 + 4) * 4);
        const u32x4 o = {cvt_pk(lo_f(v.x) * r0[0], hi_f(v.x) * r0[1]), cvt_pk(lo_f(v.y) * r0[2], hi_f(v.y) * r0[3]), cvt_pk(lo_f(v.z) * r1[0], hi_f(v.z) * r1[1]), cvt_pk(lo_f(v.w) * r1[2], hi_f(v.w) * r1[3])};
        const int hp = 4 * jp + wid, x2 = c - 18 * hh, k = x2 < 16 ? 32 * hp + 16 * hh + x2 : 512 + 4 * hp + 2 * hh + x2 - 16;
        st16_wt(rs, (unsigned)((k * S + 32 * sb2 + 8 * qd) * 2), 0, o); } }
  }
};
DI void p2_mma(const Args& a, int wid, int lane, int b) {
  typedef RingA R;
  char* ws = a.ws; unsigned* fl = (unsigned*)(ws + OFF_FLAGS);
  const P2 p = p2_of(b);
  const int w = (b & 7) * 32 + (b >> 3);
  const unsigned* gf = fl + FL_G + 36 * p.j + p.t;
  if (wid == 0) poll_words(gf, 4, (1u << GFIRST) - 1u, 1u, fl + FL_TMO, 5u, lane);
  bar();
  typedef Mma<R, 9, 2, true> M;
  M m; m.init(lane, 0, 2 * wid);
  typename M::Frag f0; unsigned so = 0;
  bar();
  m.cur = m.addr(0); m.rd(f0, m.cur, 0);
  unsigned gv = 1u;
  if (wid == 0) asm volatile("global_load_dword %0, %1, off sc1" : "=&v"(gv) : "v"(gf + 4 * (lane < 9 ? lane : 0)) : "memory");
  mma_pass<R, false>(m, f0, so, 2);
  asm volatile("s_waitcnt vmcnt(0)" : "+v"(gv) :: "memory");
  if (wid == 0 && !__all(gv == 1u)) poll_words(gf, 4, 0x1ffu, 1u, fl + FL_TMO, 6u, lane);
  mma_pass<R, false>(m, f0, so, 1);
  if (wid == 0 && lane == 0) add_relaxed(fl + FL_PREP + (w & 7) * 32);
  constexpr int PB = NG0 - 3;
  mma_pass<R, false>(m, f0, so, PB - 1 - 4);
  unsigned pv = 32u;
  if (wid == 0) asm volatile("global_load_dword %0, %1, off sc1" : "=&v"(pv) : "v"(fl + FL_PREP + (lane & 7) * 32) : "memory");
  mma_pass<R, false>(m, f0, so, 1);
  if (wid == 0) {
    asm volatile("s_waitcnt vmcnt(0)" : "+v"(pv) :: "memory");
    if (!__all(pv == 32u)) poll_words(fl + FL_PREP, 32, 0xffu, 32u, fl + FL_TMO, 2u, lane);
  }
  mma_pass<R, true>(m, f0, so, p.n - (PB - 1));
  const unsigned rslot = (unsigned)(((p.n - 3) % R::NS) * R::STG);
  if (p.j == 0) p2_finish<0>(m, rslot, wid, lane, p.i, p.t, a.out, ws);
  else if (p.j == 1) p2_finish<1>(m, rslot, wid, lane, p.i, p.t, a.out, ws);
  else if (p.j == 2) p2_finish<2>(m, rslot, wid, lane, p.i, p.t, a.out, ws);
  else p2_finish<3>(m, rslot, wid, lane, p.i, p.t, a.out, ws);
}

DI void g1_load(const Args& a, int lw, int lane, int ft, int dt) {
  typedef RingA R;
  LoadOff<R> o; load_off<R>(o, lw, lane, D, dt * 144, F * 2, ft * 256);
  const SrcLin src{mk_rsrc(a.ws + OFF_HT, S * D * 2), mk_rsrc(a.w1, (unsigned)S * F * 2), 8 * D * 16, 64 * F * 2};
  ring_run<R, 0>(o, src, lw, S / 64, NoTail());
  bar();
}
DI void g1_mma(const Args& a, int wid, int lane, int b, int ft, int dt) {
  typedef RingA R;
  char* ws = a.ws;
  typedef Mma<R, 9, 2, false> M;
  M m; m.init(lane, 0, 2 * wid);
  typename M::Frag f0; unsigned so = 0;
  const int g = lane >> 4, l15 = lane & 15;
  u32x2 bq[2];
#pragma unroll
  for (int u = 0; u < 2; u++) bq[u] = *(const u32x2*)(a.b1 + ft * 128 + 32 * wid + 16 * u + 4 * g);
  asm volatile("" : "+v"(bq[0]), "+v"(bq[1]));
  barl();
  m.cur = m.addr(0); m.rd(f0, m.cur, 0);
  mma_pass<R, true>(m, f0, so, S / 64);
  bar();
  Pqs pq; pq.init(a, wid, lane, b);
  unsigned fv;
  f32x2 bb[2][2];
#pragma unroll
  for (int u = 0; u < 2; u++) { bb[u][0] = (f32x2){lo_f(bq[u].x), hi_f(bq[u].x)}; bb[u][1] = (f32x2){lo_f(bq[u].y), hi_f(bq[u].y)}; }
  const rsrc_t rg = mk_rsrc(ws + OFF_GX, GX_BYTES);
  const unsigned go = (unsigned)((((ft * 4 + wid) * 4 + g) * D + dt * 144 + l15) * 16);
#pragma unroll
  for (int x = 0; x < 9; x++) {
    const f32x4 p = m.acc[x][0], c = m.acc[x][1];
    st16_wt(rg, go + x * 256, 0, (u32x4){gelu_pk((f32x2){p[0], p[1]} + bb[0][0]), gelu_pk((f32x2){p[2], p[3]} + bb[0][1]), gelu_pk((f32x2){c[0], c[1]} + bb[1][0]), gelu_pk((f32x2){c[2], c[3]} + bb[1][1])});
    if (x == 5) { __builtin_amdgcn_sched_barrier(0); asm volatile("global_load_dword %0, %1, off sc1" : "=&v"(fv) : "v"(pq.flag_ptr()) : "memory"); __builtin_amdgcn_sched_barrier(0); }
  }
  asm volatile("s_waitcnt vmcnt(3)" : "+v"(fv) :: "memory");
  auto flag = [&]() { if (wid == 0 && lane == 0) st_relaxed((unsigned*)(ws + OFF_FLAGS) + FL_G + ft * 4 + dt, 1u); };
  if (__all(fv == 1u)) { pq.issue(); lds_signal(CTR_S, lane); wait_vm<Pqs::NLOADS>(); quad_barrier(CTR_C, 1, lane); pq.wait(); flag(); pq.compute(); }
  else { lds_signal(CTR_S, lane); wait_vm<0>(); quad_barrier(CTR_C, 1, lane); flag(); pq.poll(); pq.issue(); pq.finish(); }
}

DI void q_load(const Args& a, int lw, int lane, int b, int i, int u3) {
  typedef RingB R;
  LoadOff<R> o; load_off<R>(o, lw, lane, S, i * 128, 3456, u3 * 384);
  const SrcLin src{mk_rsrc(a.ws + OFF_XN, S * D * 2), mk_rsrc(a.wqkv, 576u * 3456u), 8 * S * 16, 64 * 3456};
  ring_run<R, 0>(o, src, lw, 9, NoTail());
  const int xcd = b & 7, gw = ((i & 1) * 3 + u3) * 4 + lw;
  warm((const char*)a.w1, 0, W1_UNITS, gw, 24, lane, MapW1{xcd});
  warm((const char*)a.w2, 3 * 16, W2_QST * 16, gw, 24, lane, MapW2{xcd});
  bar();
}
DI void q_mma(const Args& a, int wid, int lane, int b, int i, int u3) {
  typedef RingB R;
  char* ws = a.ws;
  typedef Mma<R, 4, 6, true> M;
  const int xh = wid & 1, wh = wid >> 1, g = lane >> 4, l15 = lane & 15;
  M m; m.init(lane, 4 * xh, 0);
  {
    const int q = l15 >> 2, p = lane & 3, f = ((4 * g + q) >> 1) & 3;
    const unsigned wrow = (unsigned)(R::XBYTES + (4 * g + q) * 384 + p * 8);
#pragma unroll
    for (int u = 0; u < 4; u++) { const int cb = 6 * wh + u; m.wa[u] = wrow + (cb >> 2) * 128 + (((cb & 3) ^ f) * 32); }
  }
  typename M::Frag f0; unsigned so = 0;
  barl();
  m.cur = m.addr(0); m.rd(f0, m.cur, 0);
  mma_pass<R, true>(m, f0, so, 9);
  const rsrc_t rq = mk_rsrc(ws + OFF_PQ, S * D * 2);
#pragma unroll
  for (int x = 0; x < 4; x++)
#pragma unroll
    for (int u = 0; u < 6; u++) {
      const f32x4 v = m.acc[x][u];
      const int sb = i * 8 + 4 * xh + x, c = u3 * 192 + (6 * wh + u) * 16 + l15;
      st8_wt(rq, (unsigned)((sb * D + c) * 32 + g * 8), 0, (u32x2){phi_pk(v[0], v[1]), phi_pk(v[2], v[3])});
    }
  wait_vm<0>();
  quad_barrier(CTR_C, 1, lane);
  if (wid == 0 && lane == 0) st_relaxed((unsigned*)(ws + OFF_FLAGS) + FL_QKV + 64 + i * 3 + u3, 1u);
  bar();
  Pqs pq; pq.init(a, wid, lane, b); pq.poll(); pq.issue(); pq.finish();
}

DI void kv_load(const Args& a, int lw, int lane, int i, int j) {
  typedef RingB R;
  LoadOff<R> o; load_off<R>(o, lw, lane, S, i * 128, 3456, 1152 + j * 256);
  const SrcKV src{mk_rsrc(a.ws + OFF_XN, S * D * 2), mk_rsrc(a.wqkv, 576u * 3456u)};
  ring_run<R, 0>(o, src, lw, 18, NoTail());
  bar(); bar(); bar(); bar();
}
DI void kv_mma(const Args& a, int wid, int lane, int b, int i, int j) {
  typedef RingB R;
  char* ws = a.ws;
  typedef Mma<R, 2, 9, true> M;
  M m; m.init(lane, 2 * wid, j);
  typename M::Frag f0; unsigned so = 0;
  barl();
  m.cur = m.addr(0); m.rd(f0, m.cur, 0);
  mma_pass<R, false>(m, f0, so, 9);
#pragma unroll
  for (int u = 0; u < 9; u++) {
    const f32x4 p = m.acc[0][u], c = m.acc[1][u];
    lds_st<u32x4>(KOFF + u * 4096 + wid * 1024 + lane * 16, (u32x4){phi_pk(p[0], p[1]), phi_pk(p[2], p[3]), phi_pk(c[0], c[1]), phi_pk(c[2], c[3])});
  }
  m.zero();
  mma_pass<R, true>(m, f0, so, 9);
  barl();
  constexpr unsigned VOFF = 0;
#pragma unroll
  for (int u = 0; u < 9; u++) {
    const f32x4 p = m.acc[0][u], c = m.acc[1][u];
    lds_st<u32x4>(VOFF + u * 4096 + wid * 1024 + lane * 16, (u32x4){cvt_pk(p[0], p[1]), cvt_pk(p[2], p[3]), cvt_pk(c[0], c[1]), cvt_pk(c[2], c[3])});
  }
  barl();
  const int g = lane >> 4, l15 = lane & 15;
  const s16x8 ones = {0x3f80, 0x3f80, 0x3f80, 0x3f80, 0x3f80, 0x3f80, 0x3f80, 0x3f80};
  constexpr unsigned IMG = 40960;
  const rsrc_t rz = mk_rsrc(ws + OFF_ZS, 64u * 144u * 4u), rk = mk_rsrc(ws + OFF_KVS, 64u * KVS_SLAB * 4u);
  for (int bi = wid; bi < 9; bi += 4) {
    f32x4 dz = {0.f, 0.f, 0.f, 0.f}, d0 = dz, d1 = dz, d2 = dz;
    const int bl = bi > 0 ? bi - 1 : 0, bh = bi < 8 ? bi + 1 : 8;
#pragma unroll
    for (int sw = 0; sw < 4; sw++) {
      const s16x8 kf = lds_rd128(KOFF + bi * 4096 + sw * 1024 + lane * 16);
      dz = mfma16(ones, kf, dz);
      d0 = mfma16(lds_rd128(VOFF + bl * 4096 + sw * 1024 + lane * 16), kf, d0);
      d1 = mfma16(lds_rd128(VOFF + bi * 4096 + sw * 1024 + lane * 16), kf, d1);
      d2 = mfma16(lds_rd128(VOFF + bh * 4096 + sw * 1024 + lane * 16), kf, d2);
    }
    const unsigned ia = IMG + ((16 * bi + l15) * 52 + 4 * g) * 4;
    lds_st<f32x4>(ia, d0); lds_st<f32x4>(ia + 64, d1); lds_st<f32x4>(ia + 128, d2);
    if (g == 0) st4_wt(rz, (unsigned)(((i * 4 + j) * 144 + 16 * bi + l15) * 4), 0, __float_as_uint(dz[0]));
  }
  barl();
#pragma unroll
  for (int m3 = 0; m3 < 3; m3++) {
    const int idx = wid * 64 + lane + 256 * m3;
    if (idx < 720) { const int dd = idx / 5, cq = idx - 5 * dd, hl = dd / 18;
      const unsigned ad = IMG + (dd * 52 + 18 * hl - 16 * ((dd >> 4) - 1) + 4 * cq) * 4;
      const u32x2 v0 = lds_ld<u32x2>(ad), v1 = lds_ld<u32x2>(ad + 8);
      st16_wt(rk, (unsigned)((i * 4 + j) * (KVS_SLAB * 4) + idx * 16), 0, (u32x4){v0.x, v0.y, v1.x, v1.y}); }
  }
  wait_vm<0>();
  wait_lgkm0();
  bar();
  if (wid == 0 && lane == 0) st_relaxed((unsigned*)(ws + OFF_FLAGS) + FL_QKV + i * 4 + j, 1u);
  Pqs pq; pq.init(a, wid, lane, b); pq.poll(); pq.issue(); pq.finish();
}
}

extern "C" __global__ __launch_bounds__(512, 2) void psum_k2(const u16* __restrict__ x, const u16* __restrict__ wqkv, const u16* __restrict__ wo, const u16* __restrict__ w1, const u16* __restrict__ b1,
                                                             const u16* __restrict__ w2, const u16* __restrict__ b2, u16* __restrict__ out, char* __restrict__ ws) {
  extern __shared__ __attribute__((aligned(16))) char smem[];
  asm volatile("" ::"s"(x), "s"(wqkv), "s"(wo), "s"(w1), "s"(b1), "s"(w2), "s"(b2), "s"(out), "s"(ws));
  const int tid = threadIdx.x, wid = rfl(tid >> 6), lane = tid & 63;
  const int b = blockIdx.x, xcd = b & 7, r = b >> 3;
  const Args a{x, wqkv, wo, w1, b1, w2, b2, out, ws};
  const int gi = xcd * 18 + r;
  const int kvi = 8 * (xcd >> 2) + (r - 18), kvj = xcd & 3;
  const int qi = 2 * xcd + ((r - 26) & 1), qu = (r - 26) >> 1;
  if (wid >= 4) {
    const int lw = wid - 4;
    __builtin_amdgcn_s_setprio(1);
    if (r < 18) g1_load(a, lw, lane, gi >> 2, gi & 3);
    else if (r < 26) kv_load(a, lw, lane, kvi, kvj);
    else q_load(a, lw, lane, b, qi, qu);
    p2_load(a, lw, lane, b, r < 18);
  } else {
    if (tid == 0) { lds_st<unsigned>(CTR_C, 0u); lds_st<unsigned>(CTR_L, 0u); lds_st<unsigned>(CTR_S, 0u); }
    if (r < 18) g1_mma(a, wid, lane, b, gi >> 2, gi & 3);
    else if (r < 26) kv_mma(a, wid, lane, b, kvi, kvj);
    else q_mma(a, wid, lane, b, qi, qu);
    p2_mma(a, wid, lane, b);
  }
}

extern "C" void kernel_launch(void* const* d_in, const int* in_sizes, int n_in, void* d_out, int out_size, void* d_ws, size_t ws_size, hipStream_t stream) {
  const u16* x = (const u16*)d_in[0];
  (void)hipFuncSetAttribute((const void*)psum_k2, hipFuncAttributeMaxDynamicSharedMemorySize, LDS_ALL);
  psum_k1_ln<<<256, 256, 0, stream>>>(x, (const char*)d_in[1], (const char*)d_in[2], (const char*)d_in[4], (char*)d_ws);
  psum_k2<<<256, 512, LDS_ALL, stream>>>(x, (const u16*)d_in[1], (const u16*)d_in[2], (const u16*)d_in[4], (const u16*)d_in[5], (const u16*)d_in[6], (const u16*)d_in[7],
                                         (u16*)d_out, (char*)d_ws);
}
```
